```python
import torch

def _softmax(x, axis=-1):
    return torch.softmax(x, dim=axis)
def _gelu(x):
    return torch.nn.functional.gelu(x, approximate="tanh")
def _silu(x):
    return torch.nn.functional.silu(x)
def _sigmoid(x):
    return torch.sigmoid(x)
def _layernorm(x, axis=-1, eps=1e-5):
    m = x.mean(dim=axis, keepdim=True)
    v = x.var(dim=axis, keepdim=True, unbiased=False)
    return (x - m) / torch.sqrt(v + eps)
def _scatter_add(tgt, idx, src, axis=0):
    return tgt.clone().index_add_(axis, idx, src)
def _segment_sum(src, seg, n):
    out = torch.zeros((n,) + src.shape[1:], dtype=src.dtype)
    return out.index_add_(0, seg, src)
def _pad(x, pw):
    flat = tuple(v for pair in reversed(pw) for v in pair)
    return torch.nn.functional.pad(x, flat)

kernel_name = 'concat_heads__sliding_block_cached__sliding_block_cached__decode'

def setup_inputs():

    _w = (lambda a: (lambda t: t.to(torch.bfloat16) if t.is_floating_point() else t)(torch.as_tensor(a)))
    def _A_setup():
        B, Sq, D = 4, 1, 512
        Hq, Hkv, Sk, Dh = 32, 8, 8192, 128
        assert Hq % Hkv == 0, (Hq, Hkv)
        return {
            "x": _w((lambda *s: torch.randn(*s, dtype=torch.bfloat16) * 0.8)(B, Sq, D)),
            "k_cache": _w((lambda *s: torch.randn(*s, dtype=torch.bfloat16) * 1.6)(B, Hkv, Sk, Dh)),
            "v_cache": _w((lambda *s: torch.randn(*s, dtype=torch.bfloat16) * 0.2)(B, Hkv, Sk, Dh)),
            "wq": _w((lambda *s: torch.randn(*s, dtype=torch.bfloat16) * 1.6)(D, Hq * Dh) * (D ** -0.5)),
            "wo": _w((lambda *s: torch.randn(*s, dtype=torch.bfloat16) * 0.2)(Hq * Dh, D) * ((Hq * Dh) ** -0.5)),
        }
    _A_in = _A_setup()

    def _B_setup():
        B, Sq, D = 4, 1, 512
        Hq, Hkv, Sk, Dh = 32, 8, 8192, 128
        assert Hq % Hkv == 0, (Hq, Hkv)
        return {
            "x": _w((lambda *s: torch.randn(*s, dtype=torch.bfloat16) * 0.8)(B, Sq, D)),
            "k_cache": _w((lambda *s: torch.randn(*s, dtype=torch.bfloat16) * 1.6)(B, Hkv, Sk, Dh)),
            "v_cache": _w((lambda *s: torch.randn(*s, dtype=torch.bfloat16) * 0.2)(B, Hkv, Sk, Dh)),
            "wq": _w((lambda *s: torch.randn(*s, dtype=torch.bfloat16) * 1.6)(D, Hq * Dh) * (D ** -0.5)),
            "wo": _w((lambda *s: torch.randn(*s, dtype=torch.bfloat16) * 0.2)(Hq * Dh, D) * ((Hq * Dh) ** -0.5)),
        }
    _B_in = _B_setup()

    return {
        "A_x": _A_in["x"],
        "A_k_cache": _A_in["k_cache"],
        "A_v_cache": _A_in["v_cache"],
        "A_wq": _A_in["wq"],
        "A_wo": _A_in["wo"],
        "B_x": _B_in["x"],
        "B_k_cache": _B_in["k_cache"],
        "B_v_cache": _B_in["v_cache"],
        "B_wq": _B_in["wq"],
        "B_wo": _B_in["wo"],
    }

def reference(A_x, A_k_cache, A_v_cache, A_wq, A_wo, B_x, B_k_cache, B_v_cache, B_wq, B_wo):
    def _A_ref(x, k_cache, v_cache, wq, wo):
        B, Sq, D = x.shape
        Hq, Dh = 32, 128
        Hkv, Sk = k_cache.shape[1], k_cache.shape[2]
        W = min(4096, max(256, Sk // 4))
        rep = Hq // Hkv
        q = (lambda x, *d: x.permute(*d))((x @ wq).reshape(B, Sq, Hq, Dh), 0, 2, 1, 3)
        k = (lambda x, n, axis: torch.repeat_interleave(x, n, dim=axis))(k_cache[:, :, Sk - W:, :], rep, axis=1)
        v = (lambda x, n, axis: torch.repeat_interleave(x, n, dim=axis))(v_cache[:, :, Sk - W:, :], rep, axis=1)
        scores = torch.einsum("bhqd,bhkd->bhqk", q, k) * (Dh ** -0.5)
        p = _softmax(scores, axis=-1)
        o = torch.einsum("bhqk,bhkd->bhqd", p, v)
        return (lambda x, *d: x.permute(*d))(o, 0, 2, 1, 3).reshape(B, Sq, Hq * Dh) @ wo

    def _B_ref(x, k_cache, v_cache, wq, wo):
        B, Sq, D = x.shape
        Hq, Dh = 32, 128
        Hkv, Sk = k_cache.shape[1], k_cache.shape[2]
        W = min(4096, max(256, Sk // 4))
        rep = Hq // Hkv
        q = (lambda x, *d: x.permute(*d))((x @ wq).reshape(B, Sq, Hq, Dh), 0, 2, 1, 3)
        k = (lambda x, n, axis: torch.repeat_interleave(x, n, dim=axis))(k_cache[:, :, Sk - W:, :], rep, axis=1)
        v = (lambda x, n, axis: torch.repeat_interleave(x, n, dim=axis))(v_cache[:, :, Sk - W:, :], rep, axis=1)
        scores = torch.einsum("bhqd,bhkd->bhqk", q, k) * (Dh ** -0.5)
        p = _softmax(scores, axis=-1)
        o = torch.einsum("bhqk,bhkd->bhqd", p, v)
        return (lambda x, *d: x.permute(*d))(o, 0, 2, 1, 3).reshape(B, Sq, Hq * Dh) @ wo

    a = _A_ref(A_x, A_k_cache, A_v_cache, A_wq, A_wo)
    b = _B_ref(B_x, B_k_cache, B_v_cache, B_wq, B_wo)
    return torch.concatenate([a, b], axis=-1)
```

```cpp
#include <hip/hip_runtime.h>
#include <hip/hip_bf16.h>
#include <stdint.h>

typedef unsigned u32x4 __attribute__((ext_vector_type(4)));
typedef float f32x4 __attribute__((ext_vector_type(4)));
typedef __attribute__((ext_vector_type(2))) __bf16 bf16x2;

constexpr int NB = 4, DM = 512, HQ = 32, HKV = 8, SK = 8192, DH = 128, WIN = 2048;
constexpr int NSPLIT = 4, KPS = WIN / NSPLIT;
constexpr float SCALE = 0.08838834764831845f;
constexpr float LOG2E = 1.4426950408889634f;

constexpr size_t WS_Q = 0;
constexpr size_t WS_PO = 65536;
constexpr size_t WS_ML = WS_PO + (size_t)2 * 4 * 8 * 4 * 4 * 128 * 4;
constexpr size_t WS_Y = WS_ML + (size_t)2 * 4 * 8 * 4 * 4 * 2 * 4;
constexpr size_t WS_END = WS_Y + (size_t)2 * 128 * 4 * 512 * 4;

__device__ __forceinline__ float bf2f(unsigned short h) { return __uint_as_float(((unsigned)h) << 16); }
__device__ __forceinline__ float bflo(unsigned u) { return __uint_as_float(u << 16); }
__device__ __forceinline__ float bfhi(unsigned u) { return __uint_as_float(u & 0xffff0000u); }
__device__ __forceinline__ float rbf(float f) { __bf16 h = (__bf16)f; return (float)h; }
__device__ __forceinline__ unsigned short f2bf(float f) { __bf16 h = (__bf16)f; return __builtin_bit_cast(unsigned short, h); }
__device__ __forceinline__ float dot2(unsigned k, unsigned q, float acc) {
  return __builtin_amdgcn_fdot2_f32_bf16(__builtin_bit_cast(bf16x2, k), __builtin_bit_cast(bf16x2, q), acc, false);
}
template <int CTRL> __device__ __forceinline__ float dpp(float x) {
  return __builtin_bit_cast(float, __builtin_amdgcn_mov_dpp(__builtin_bit_cast(int, x), CTRL, 0xf, 0xf, true));
}
constexpr int XOR1 = 0xB1, XOR2 = 0x4E, XOR7 = 0x141, XOR8 = 0x128, ROR4 = 0x124;
__device__ __forceinline__ float xrow16_max(float x) {
  auto s = __builtin_amdgcn_permlane16_swap(__float_as_uint(x), __float_as_uint(x), false, false);
  x = fmaxf(__uint_as_float(s[0]), __uint_as_float(s[1]));
  auto t = __builtin_amdgcn_permlane32_swap(__float_as_uint(x), __float_as_uint(x), false, false);
  return fmaxf(__uint_as_float(t[0]), __uint_as_float(t[1]));
}
__device__ __forceinline__ float xrow16_sum(float x) {
  auto s = __builtin_amdgcn_permlane16_swap(__float_as_uint(x), __float_as_uint(x), false, false);
  x = __uint_as_float(s[0]) + __uint_as_float(s[1]);
  auto t = __builtin_amdgcn_permlane32_swap(__float_as_uint(x), __float_as_uint(x), false, false);
  return __uint_as_float(t[0]) + __uint_as_float(t[1]);
}
__device__ __forceinline__ u32x4 ldnt(const void* p) { return __builtin_nontemporal_load(reinterpret_cast<const u32x4*>(p)); }

struct Ptrs {
  const unsigned short* x[2];
  const unsigned short* k[2];
  const unsigned short* v[2];
  const unsigned short* wq[2];
  const unsigned short* wo[2];
  unsigned short* out;
  unsigned char* ws;
};

__global__ void __launch_bounds__(512) k_qproj(Ptrs p) {
  __shared__ float red[8][4][32];
  const int wg = blockIdx.x, br = wg >> 7, tile = wg & 127;
  const int tid = threadIdx.x, lane = tid & 63, w = tid >> 6;
  const int r = lane >> 2, c = lane & 3;
  const unsigned short* wq = p.wq[br];
  const unsigned short* x = p.x[br];
  u32x4 W[4];
#pragma unroll
  for (int t = 0; t < 4; ++t) W[t] = ldnt(wq + (size_t)(64 * w + 16 * t + r) * 4096 + tile * 32 + 8 * c);
  float acc[4][8];
#pragma unroll
  for (int b = 0; b < 4; ++b)
#pragma unroll
    for (int k = 0; k < 8; ++k) acc[b][k] = 0.f;
#pragma unroll
  for (int t = 0; t < 4; ++t) {
    const int row = 64 * w + 16 * t + r;
#pragma unroll
    for (int b = 0; b < 4; ++b) {
      const float xv = bf2f(x[b * DM + row]);
#pragma unroll
      for (int k = 0; k < 4; ++k) {
        acc[b][2 * k] = fmaf(xv, bflo(W[t][k]), acc[b][2 * k]);
        acc[b][2 * k + 1] = fmaf(xv, bfhi(W[t][k]), acc[b][2 * k + 1]);
      }
    }
  }
#pragma unroll
  for (int b = 0; b < 4; ++b)
#pragma unroll
    for (int k = 0; k < 8; ++k) {
      float v = acc[b][k];
      v += __shfl_xor(v, 4); v += __shfl_xor(v, 8); v += __shfl_xor(v, 16); v += __shfl_xor(v, 32);
      acc[b][k] = v;
    }
  if (r == 0) {
#pragma unroll
    for (int b = 0; b < 4; ++b)
#pragma unroll
      for (int k = 0; k < 8; ++k) red[w][b][8 * c + k] = acc[b][k];
  }
  __syncthreads();
  if (tid < 128) {
    const int b = tid >> 5, col = tid & 31;
    float s = 0.f;
#pragma unroll
    for (int ww = 0; ww < 8; ++ww) s += red[ww][b][col];
    unsigned short* q = reinterpret_cast<unsigned short*>(p.ws + WS_Q);
    q[(br * 4 + b) * 4096 + tile * 32 + col] = f2bf(s);
  }
}

__global__ void __launch_bounds__(512) k_attn(Ptrs p) {
  __shared__ __attribute__((aligned(16))) float pl[8][64][4];
  __shared__ __attribute__((aligned(16))) float accl[8][4][128];
  __shared__ float wmax[8][4], wsum[8][4];
  const int wg = blockIdx.x, br = wg >> 7, rem = wg & 127, g = rem >> 4, j = rem & 15, b = j >> 2, s = j & 3;
  const int tid = threadIdx.x, lane = tid & 63, w = tid >> 6;
  const int r = lane >> 4, c = lane & 15;
  const size_t kvbase = ((size_t)(b * HKV + g) * SK + (SK - WIN) + s * KPS + w * 64) * DH;
  const unsigned short* kp = p.k[br] + kvbase + (size_t)r * DH + 8 * c;
  const unsigned short* vp = p.v[br] + kvbase + (size_t)r * DH + 8 * c;
  u32x4 Kr[16], Vr[16];
#pragma unroll
  for (int t = 0; t < 16; ++t) Kr[t] = ldnt(kp + (size_t)t * 4 * DH);
#pragma unroll
  for (int t = 0; t < 16; ++t) Vr[t] = ldnt(vp + (size_t)t * 4 * DH);
  const unsigned short* q = reinterpret_cast<const unsigned short*>(p.ws + WS_Q) + (br * 4 + b) * 4096 + g * 512;
  u32x4 qp[4];
#pragma unroll
  for (int h = 0; h < 4; ++h) qp[h] = *reinterpret_cast<const u32x4*>(q + h * DH + 8 * c);
  float sc[4];
#pragma unroll
  for (int gi = 0; gi < 4; ++gi) {
    float x[16];
#pragma unroll
    for (int tl = 0; tl < 4; ++tl)
#pragma unroll
      for (int h = 0; h < 4; ++h) {
        float d = 0.f;
#pragma unroll
        for (int k = 0; k < 4; ++k) d = dot2(Kr[4 * gi + tl][k], qp[h][k], d);
        x[tl * 4 + h] = d;
      }
#define TR_STEP(HALF, CTRL, BIT) _Pragma("unroll") for (int hh = 0; hh < (HALF); ++hh) { \
      const float lo_ = x[hh], hi_ = x[hh + (HALF)]; const float t1_ = lo_ + dpp<CTRL>(lo_), t2_ = hi_ + dpp<CTRL>(hi_); \
      x[hh] = (c & (BIT)) ? t2_ : t1_; }
    TR_STEP(8, XOR8, 8) TR_STEP(4, XOR7, 4) TR_STEP(2, XOR2, 2) TR_STEP(1, XOR1, 1)
#undef TR_STEP
    sc[gi] = rbf(rbf(x[0]) * SCALE);
  }
  float m = fmaxf(fmaxf(sc[0], sc[1]), fmaxf(sc[2], sc[3]));
  m = fmaxf(m, dpp<ROR4>(m)); m = fmaxf(m, dpp<XOR8>(m)); m = xrow16_max(m);
  if (lane < 4) wmax[w][lane] = m;
  __syncthreads();
  float M = wmax[0][c & 3];
#pragma unroll
  for (int ww = 1; ww < 8; ++ww) M = fmaxf(M, wmax[ww][c & 3]);
  float l = 0.f;
#pragma unroll
  for (int gi = 0; gi < 4; ++gi) {
    const float pv = __builtin_amdgcn_exp2f((sc[gi] - M) * LOG2E);
    l += pv;
    pl[w][4 * (4 * gi + (c >> 2)) + r][c & 3] = pv;
  }
  l += dpp<ROR4>(l); l += dpp<XOR8>(l); l = xrow16_sum(l);
  if (lane < 4) wsum[w][lane] = l;
  __builtin_amdgcn_s_waitcnt(0xC07F);
  __builtin_amdgcn_wave_barrier();
  float acc[4][8];
#pragma unroll
  for (int h = 0; h < 4; ++h)
#pragma unroll
    for (int k = 0; k < 8; ++k) acc[h][k] = 0.f;
#pragma unroll
  for (int t = 0; t < 16; ++t) {
    const f32x4 pr = *reinterpret_cast<const f32x4*>(&pl[w][4 * t + r][0]);
    float vf[8];
#pragma unroll
    for (int k = 0; k < 4; ++k) { vf[2 * k] = bflo(Vr[t][k]); vf[2 * k + 1] = bfhi(Vr[t][k]); }
#pragma unroll
    for (int h = 0; h < 4; ++h)
#pragma unroll
      for (int k = 0; k < 8; ++k) acc[h][k] = fmaf(pr[h], vf[k], acc[h][k]);
  }
#pragma unroll
  for (int h = 0; h < 4; ++h)
#pragma unroll
    for (int k = 0; k < 8; ++k) acc[h][k] = xrow16_sum(acc[h][k]);
  if (r == 0) {
#pragma unroll
    for (int h = 0; h < 4; ++h) {
      *reinterpret_cast<f32x4*>(&accl[w][h][8 * c]) = f32x4{acc[h][0], acc[h][1], acc[h][2], acc[h][3]};
      *reinterpret_cast<f32x4*>(&accl[w][h][8 * c + 4]) = f32x4{acc[h][4], acc[h][5], acc[h][6], acc[h][7]};
    }
  }
  __syncthreads();
  {
    const int h = tid >> 7, d = tid & 127;
    float o = 0.f;
#pragma unroll
    for (int ww = 0; ww < 8; ++ww) o += accl[ww][h][d];
    const size_t slot = ((((size_t)br * 4 + b) * 8 + g) * 4 + s) * 4 + h;
    reinterpret_cast<float*>(p.ws + WS_PO)[slot * 128 + d] = o;
    if (d == 0) {
      float L = 0.f, Mh = wmax[0][h];
#pragma unroll
      for (int ww = 0; ww < 8; ++ww) { L += wsum[ww][h]; Mh = fmaxf(Mh, wmax[ww][h]); }
      float* ml = reinterpret_cast<float*>(p.ws + WS_ML) + slot * 2;
      ml[0] = Mh; ml[1] = L;
    }
  }
}

__global__ void __launch_bounds__(512) k_oproj(Ptrs p) {
  __shared__ float ol[4][32];
  __shared__ __attribute__((aligned(16))) float yl[8][4][512];
  const int wg = blockIdx.x, br = wg >> 7, rem = wg & 127, g = rem >> 4, j = rem & 15;
  const int tid = threadIdx.x, lane = tid & 63, w = tid >> 6;
  const unsigned short* wo = p.wo[br] + (size_t)(g * 512 + j * 32 + 4 * w) * DM + 8 * lane;
  u32x4 Wr[4];
#pragma unroll
  for (int i = 0; i < 4; ++i) Wr[i] = ldnt(wo + (size_t)i * DM);
  if (tid < 128) {
    const int b = tid >> 5, d = tid & 31, h = j >> 2, dq = j & 3;
    const float* po = reinterpret_cast<const float*>(p.ws + WS_PO);
    const float* ml = reinterpret_cast<const float*>(p.ws + WS_ML);
    const size_t slot0 = (((size_t)br * 4 + b) * 8 + g) * 4;
    float ms[4], ls[4], M = -INFINITY;
#pragma unroll
    for (int s = 0; s < 4; ++s) { const size_t sl = (slot0 + s) * 4 + h; ms[s] = ml[sl * 2]; ls[s] = ml[sl * 2 + 1]; M = fmaxf(M, ms[s]); }
    float L = 0.f, o = 0.f;
#pragma unroll
    for (int s = 0; s < 4; ++s) { const size_t sl = (slot0 + s) * 4 + h; const float wt = __builtin_amdgcn_exp2f((ms[s] - M) * LOG2E);
      L = fmaf(wt, ls[s], L); o = fmaf(wt, po[sl * 128 + dq * 32 + d], o); }
    ol[b][d] = rbf(o / L);
  }
  __syncthreads();
  float y[4][8];
#pragma unroll
  for (int b = 0; b < 4; ++b)
#pragma unroll
    for (int k = 0; k < 8; ++k) y[b][k] = 0.f;
#pragma unroll
  for (int i = 0; i < 4; ++i)
#pragma unroll
    for (int b = 0; b < 4; ++b) {
      const float ov = ol[b][4 * w + i];
#pragma unroll
      for (int k = 0; k < 4; ++k) { y[b][2 * k] = fmaf(ov, bflo(Wr[i][k]), y[b][2 * k]); y[b][2 * k + 1] = fmaf(ov, bfhi(Wr[i][k]), y[b][2 * k + 1]); }
    }
#pragma unroll
  for (int b = 0; b < 4; ++b) {
    *reinterpret_cast<f32x4*>(&yl[w][b][8 * lane]) = f32x4{y[b][0], y[b][1], y[b][2], y[b][3]};
    *reinterpret_cast<f32x4*>(&yl[w][b][8 * lane + 4]) = f32x4{y[b][4], y[b][5], y[b][6], y[b][7]};
  }
  __syncthreads();
  float* yo = reinterpret_cast<float*>(p.ws + WS_Y) + (size_t)wg * 2048;
#pragma unroll
  for (int i = 0; i < 4; ++i) {
    const int idx = tid + 512 * i, b = idx >> 9, col = idx & 511;
    float sum = 0.f;
#pragma unroll
    for (int ww = 0; ww < 8; ++ww) sum += yl[ww][b][col];
    yo[idx] = sum;
  }
}

__global__ void __launch_bounds__(256) k_final(Ptrs p) {
  __shared__ float red[8][32];
  const int blk = blockIdx.x;
  const int br = blk >> 6, b = (blk >> 4) & 3, cb = blk & 15;
  const int tid = threadIdx.x, sl = tid >> 5, cc = tid & 31;
  const float* y = reinterpret_cast<const float*>(p.ws + WS_Y) + (size_t)br * 128 * 2048 + b * 512 + cb * 32 + cc;
  float sum = 0.f;
#pragma unroll
  for (int i = 0; i < 16; ++i) sum += y[(size_t)(sl * 16 + i) * 2048];
  red[sl][cc] = sum;
  __syncthreads();
  if (tid < 32) {
    float t = 0.f;
#pragma unroll
    for (int i = 0; i < 8; ++i) t += red[i][tid];
    p.out[b * 1024 + br * 512 + cb * 32 + tid] = f2bf(t);
  }
}

extern "C" void kernel_launch(void* const* d_in, const int* in_sizes, int n_in,
                              void* d_out, int out_size, void* d_ws, size_t ws_size,
                              hipStream_t stream) {
  Ptrs p;
  for (int br = 0; br < 2; ++br) {
    p.x[br] = (const unsigned short*)d_in[5 * br + 0];
    p.k[br] = (const unsigned short*)d_in[5 * br + 1];
    p.v[br] = (const unsigned short*)d_in[5 * br + 2];
    p.wq[br] = (const unsigned short*)d_in[5 * br + 3];
    p.wo[br] = (const unsigned short*)d_in[5 * br + 4];
  }
  p.out = (unsigned short*)d_out;
  p.ws = (unsigned char*)d_ws;
  hipLaunchKernelGGL(k_qproj, dim3(256), dim3(512), 0, stream, p);
  hipLaunchKernelGGL(k_attn, dim3(256), dim3(512), 0, stream, p);
  hipLaunchKernelGGL(k_oproj, dim3(256), dim3(512), 0, stream, p);
  hipLaunchKernelGGL(k_final, dim3(128), dim3(256), 0, stream, p);
}
```

```cpp
#include <hip/hip_runtime.h>
#include <hip/hip_bf16.h>
#include <stdint.h>

typedef unsigned u32x4 __attribute__((ext_vector_type(4)));
typedef float f32x4 __attribute__((ext_vector_type(4)));
typedef __attribute__((ext_vector_type(2))) __bf16 bf16x2;
typedef unsigned long long u64;
#define GAS __attribute__((address_space(1)))
typedef GAS u64 gu64;
typedef GAS unsigned gu32;
#define RLX_AGENT __ATOMIC_RELAXED, __HIP_MEMORY_SCOPE_AGENT

constexpr int NB = 4, DM = 512, HQ = 32, HKV = 8, SK = 8192, DH = 128, WIN = 2048;
constexpr int NSPLIT = 4, KPS = WIN / NSPLIT;
constexpr float SCALE = 0.08838834764831845f;
constexpr float LOG2E = 1.4426950408889634f;
constexpr unsigned SPIN_CAP = 20000;

constexpr size_t WS_E = 0;
constexpr size_t WS_H1 = 4096;
constexpr size_t WS_H2 = WS_H1 + (size_t)256 * 256 * 8;
constexpr int H2S = 36;
constexpr size_t WS_H3 = WS_H2 + (size_t)256 * 16 * H2S * 8;
constexpr size_t WS_END = WS_H3 + (size_t)256 * 128 * 16 * 8;

__device__ __forceinline__ float bf2f(unsigned short h) { return __uint_as_float(((unsigned)h) << 16); }
__device__ __forceinline__ float bflo(unsigned u) { return __uint_as_float(u << 16); }
__device__ __forceinline__ float bfhi(unsigned u) { return __uint_as_float(u & 0xffff0000u); }
__device__ __forceinline__ float rbf(float f) { __bf16 h = (__bf16)f; return (float)h; }
__device__ __forceinline__ unsigned short f2bf(float f) { __bf16 h = (__bf16)f; return __builtin_bit_cast(unsigned short, h); }
__device__ __forceinline__ float dot2(unsigned k, unsigned q, float acc) {
  return __builtin_amdgcn_fdot2_f32_bf16(__builtin_bit_cast(bf16x2, k), __builtin_bit_cast(bf16x2, q), acc, false);
}
template <int CTRL> __device__ __forceinline__ float dpp(float x) {
  return __builtin_bit_cast(float, __builtin_amdgcn_mov_dpp(__builtin_bit_cast(int, x), CTRL, 0xf, 0xf, true));
}
constexpr int XOR1 = 0xB1, XOR2 = 0x4E, XOR7 = 0x141, XOR8 = 0x128, ROR4 = 0x124;
__device__ __forceinline__ float xrow16_max(float x) {
  auto s = __builtin_amdgcn_permlane16_swap(__float_as_uint(x), __float_as_uint(x), false, false);
  x = fmaxf(__uint_as_float(s[0]), __uint_as_float(s[1]));
  auto t = __builtin_amdgcn_permlane32_swap(__float_as_uint(x), __float_as_uint(x), false, false);
  return fmaxf(__uint_as_float(t[0]), __uint_as_float(t[1]));
}
__device__ __forceinline__ float xrow16_sum(float x) {
  auto s = __builtin_amdgcn_permlane16_swap(__float_as_uint(x), __float_as_uint(x), false, false);
  x = __uint_as_float(s[0]) + __uint_as_float(s[1]);
  auto t = __builtin_amdgcn_permlane32_swap(__float_as_uint(x), __float_as_uint(x), false, false);
  return __uint_as_float(t[0]) + __uint_as_float(t[1]);
}
__device__ __forceinline__ u32x4 ldnt(const void* p) { return __builtin_nontemporal_load(reinterpret_cast<const u32x4*>(p)); }
__device__ __forceinline__ void st_gran(unsigned char* ws, size_t byteoff, unsigned tag, unsigned val) {
  __hip_atomic_store((gu64*)(ws + byteoff), ((u64)tag << 32) | (u64)val, RLX_AGENT);
}
__device__ __forceinline__ u64 ld_gran(const unsigned char* ws, size_t byteoff) {
  return __hip_atomic_load((gu64*)(ws + byteoff), RLX_AGENT);
}
__device__ __forceinline__ void wg_barrier() {
  asm volatile("s_waitcnt lgkmcnt(0)" ::: "memory");
  __builtin_amdgcn_s_barrier();
  asm volatile("" ::: "memory");
}

#define SB() __builtin_amdgcn_sched_barrier(0)
struct Ptrs {
  const unsigned short* x[2];
  const unsigned short* k[2];
  const unsigned short* v[2];
  const unsigned short* wq[2];
  const unsigned short* wo[2];
  unsigned short* out;
  unsigned char* ws;
};

__global__ void __launch_bounds__(512, 2) k_fused(Ptrs p) {
  __shared__ __attribute__((aligned(16))) float lds[8 * 4 * 512];
  __shared__ __attribute__((aligned(16))) unsigned qlds[256];
  __shared__ unsigned elds[128];
  __shared__ float wml[8][4][2];
  __shared__ float h2l[16][H2S];
  __shared__ float ol[4][32];
  __shared__ float fin[32][16];

  const int bid = blockIdx.x;
  const int role = (bid & 7) * 32 + (bid >> 3);
  const int br = role >> 7, rem = role & 127, g = rem >> 4, j = rem & 15, b = j >> 2, s = j & 3;
  const int tid = threadIdx.x, lane = tid & 63;
  const int w = __builtin_amdgcn_readfirstlane(tid >> 6);
  unsigned char* ws = p.ws;
  const int cbase = br * 128 + g * 16;

  const unsigned eword = __hip_atomic_load((gu32*)(ws + WS_E) + br * 128 + (tid & 127), RLX_AGENT);

  const int qr = lane >> 2, qc = lane & 3;
  u32x4 W[4];
  {
    const unsigned short* wq = p.wq[br] + (size_t)(64 * w + qr) * 4096 + g * 512 + j * 32 + 8 * qc;
#pragma unroll
    for (int t = 0; t < 4; ++t) W[t] = ldnt(wq + (size_t)(16 * t) * 4096);
  }
  unsigned short xr[4][4];
  {
    const unsigned short* x = p.x[br] + 64 * w + qr;
#pragma unroll
    for (int t = 0; t < 4; ++t)
#pragma unroll
      for (int bb = 0; bb < 4; ++bb) xr[t][bb] = x[bb * DM + 16 * t];
  }
  const int r = lane >> 4, c = lane & 15;
  const size_t kvbase = ((size_t)(b * HKV + g) * SK + (SK - WIN) + s * KPS + w * 64) * DH;
  const unsigned short* kp = p.k[br] + kvbase + (size_t)r * DH + 8 * c;
  const unsigned short* vp = p.v[br] + kvbase + (size_t)r * DH + 8 * c;
  u32x4 Kr[16], Vr[16];
  asm volatile("" ::: "memory");
  SB();
#pragma unroll
  for (int t = 0; t < 16; ++t) { Kr[t] = ldnt(kp + (size_t)t * 4 * DH); SB(); }
  asm volatile("" ::: "memory");
  {
    float xv[4][4];
#pragma unroll
    for (int t = 0; t < 4; ++t)
#pragma unroll
      for (int bb = 0; bb < 4; ++bb) xv[t][bb] = bf2f(xr[t][bb]);
    if (tid < 128) elds[tid] = eword;
    float acc[4][8];
#pragma unroll
    for (int bb = 0; bb < 4; ++bb)
#pragma unroll
      for (int k = 0; k < 8; ++k) acc[bb][k] = 0.f;
#pragma unroll
    for (int t = 0; t < 4; ++t)
#pragma unroll
      for (int bb = 0; bb < 4; ++bb)
#pragma unroll
        for (int k = 0; k < 4; ++k) {
          acc[bb][2 * k] = fmaf(xv[t][bb], bflo(W[t][k]), acc[bb][2 * k]);
          acc[bb][2 * k + 1] = fmaf(xv[t][bb], bfhi(W[t][k]), acc[bb][2 * k + 1]);
        }
#pragma unroll
    for (int bb = 0; bb < 4; ++bb)
#pragma unroll
      for (int k = 0; k < 8; ++k) {
        float v = acc[bb][k];
        v += __shfl_xor(v, 4); v += __shfl_xor(v, 8); v += __shfl_xor(v, 16); v += __shfl_xor(v, 32);
        acc[bb][k] = v;
      }
    if (qr == 0) {
      float* red = lds + w * 128;
#pragma unroll
      for (int bb = 0; bb < 4; ++bb)
#pragma unroll
        for (int k = 0; k < 8; ++k) red[bb * 32 + 8 * qc + k] = acc[bb][k];
    }
  }
  wg_barrier();
  if (tid < 256) {
    const int cc = tid >> 4, i = tid & 15, bb = cc >> 2;
    float s0 = 0.f, s1 = 0.f;
#pragma unroll
    for (int ww = 0; ww < 8; ++ww) { s0 += lds[ww * 128 + bb * 32 + 2 * i]; s1 += lds[ww * 128 + bb * 32 + 2 * i + 1]; }
    const unsigned val = (unsigned)f2bf(s0) | ((unsigned)f2bf(s1) << 16);
    st_gran(ws, WS_H1 + ((size_t)(cbase + cc) * 256 + j * 16 + i) * 8, elds[g * 16 + cc] + 1u, val);
  }
  asm volatile("" ::: "memory");
  const unsigned mytag = elds[g * 16 + j] + 1u;
  if (w != 0) {
    SB();
#pragma unroll
    for (int t = 0; t < 16; ++t) { Vr[t] = ldnt(vp + (size_t)t * 4 * DH); SB(); }
  } else {
    const size_t base = WS_H1 + (size_t)role * 256 * 8;
    unsigned vals[4];
    for (unsigned spin = 0; spin < SPIN_CAP; ++spin) {
      bool ok = true;
#pragma unroll
      for (int k = 0; k < 4; ++k) { const u64 x = ld_gran(ws, base + (size_t)(k * 64 + lane) * 8); vals[k] = (unsigned)x; ok &= ((unsigned)(x >> 32) == mytag); }
      if (__all(ok)) break;
      __builtin_amdgcn_s_sleep(1);
    }
#pragma unroll
    for (int k = 0; k < 4; ++k) qlds[k * 64 + lane] = vals[k];
    asm volatile("" ::: "memory");
    SB();
#pragma unroll
    for (int t = 0; t < 16; ++t) { Vr[t] = ldnt(vp + (size_t)t * 4 * DH); SB(); }
  }
  wg_barrier();
  u32x4 Wr[4];
  {
    const unsigned short* wo = p.wo[br] + (size_t)(g * 512 + j * 32 + 4 * w) * DM + 8 * lane;
#pragma unroll
    for (int i = 0; i < 4; ++i) Wr[i] = ldnt(wo + (size_t)i * DM);
  }
  SB();
  asm volatile("" ::: "memory");
  u32x4 qp[4];
#pragma unroll
  for (int h = 0; h < 4; ++h) qp[h] = *reinterpret_cast<const u32x4*>(&qlds[h * 64 + 4 * c]);
  float sc[4];
#pragma unroll
  for (int gi = 0; gi < 4; ++gi) {
    float x[16];
#pragma unroll
    for (int tl = 0; tl < 4; ++tl)
#pragma unroll
      for (int h = 0; h < 4; ++h) {
        float d = 0.f;
#pragma unroll
        for (int k = 0; k < 4; ++k) d = dot2(Kr[4 * gi + tl][k], qp[h][k], d);
        x[tl * 4 + h] = d;
      }
#define TR_STEP(HALF, CTRL, BIT) _Pragma("unroll") for (int hh = 0; hh < (HALF); ++hh) { \
      const float lo_ = x[hh], hi_ = x[hh + (HALF)]; const float t1_ = lo_ + dpp<CTRL>(lo_), t2_ = hi_ + dpp<CTRL>(hi_); \
      x[hh] = (c & (BIT)) ? t2_ : t1_; }
    TR_STEP(8, XOR8, 8) TR_STEP(4, XOR7, 4) TR_STEP(2, XOR2, 2) TR_STEP(1, XOR1, 1)
#undef TR_STEP
    sc[gi] = rbf(rbf(x[0]) * SCALE);
  }
  float m = fmaxf(fmaxf(sc[0], sc[1]), fmaxf(sc[2], sc[3]));
  m = fmaxf(m, dpp<ROR4>(m)); m = fmaxf(m, dpp<XOR8>(m)); m = xrow16_max(m);
  float* pl = lds + w * 256;
  float l = 0.f;
#pragma unroll
  for (int gi = 0; gi < 4; ++gi) {
    const float pv = __builtin_amdgcn_exp2f((sc[gi] - m) * LOG2E);
    l += pv;
    pl[(4 * (4 * gi + (c >> 2)) + r) * 4 + (c & 3)] = pv;
  }
  l += dpp<ROR4>(l); l += dpp<XOR8>(l); l = xrow16_sum(l);
  if (lane < 4) { wml[w][lane][0] = m; wml[w][lane][1] = l; }
  asm volatile("s_waitcnt lgkmcnt(0)" ::: "memory");
  __builtin_amdgcn_wave_barrier();
  float acc[4][8];
#pragma unroll
  for (int h = 0; h < 4; ++h)
#pragma unroll
    for (int k = 0; k < 8; ++k) acc[h][k] = 0.f;
#pragma unroll
  for (int t = 0; t < 16; ++t) {
    const f32x4 pr = *reinterpret_cast<const f32x4*>(&pl[(4 * t + r) * 4]);
    float vf[8];
#pragma unroll
    for (int k = 0; k < 4; ++k) { vf[2 * k] = bflo(Vr[t][k]); vf[2 * k + 1] = bfhi(Vr[t][k]); }
#pragma unroll
    for (int h = 0; h < 4; ++h)
#pragma unroll
      for (int k = 0; k < 8; ++k) acc[h][k] = fmaf(pr[h], vf[k], acc[h][k]);
  }
#pragma unroll
  for (int h = 0; h < 4; ++h)
#pragma unroll
    for (int k = 0; k < 8; ++k) acc[h][k] = xrow16_sum(acc[h][k]);
  float* accl = lds + 2048;
  if (r == 0) {
#pragma unroll
    for (int h = 0; h < 4; ++h) {
      *reinterpret_cast<f32x4*>(&accl[(w * 4 + h) * 128 + 8 * c]) = f32x4{acc[h][0], acc[h][1], acc[h][2], acc[h][3]};
      *reinterpret_cast<f32x4*>(&accl[(w * 4 + h) * 128 + 8 * c + 4]) = f32x4{acc[h][4], acc[h][5], acc[h][6], acc[h][7]};
    }
  }
  wg_barrier();
  {
    const int jc = tid >> 5, d = tid & 31, h = jc >> 2, dq = jc & 3;
    float M = wml[0][h][0];
#pragma unroll
    for (int ww = 1; ww < 8; ++ww) M = fmaxf(M, wml[ww][h][0]);
    float o = 0.f, L = 0.f;
#pragma unroll
    for (int ww = 0; ww < 8; ++ww) {
      const float wt = __builtin_amdgcn_exp2f((wml[ww][h][0] - M) * LOG2E);
      o = fmaf(wt, accl[(ww * 4 + h) * 128 + dq * 32 + d], o);
      L = fmaf(wt, wml[ww][h][1], L);
    }
    const unsigned tag = elds[g * 16 + jc] + 1u;
    const size_t base = WS_H2 + ((size_t)(cbase + jc) * 16 + j) * H2S * 8;
    st_gran(ws, base + (size_t)d * 8, tag, __float_as_uint(o));
    if (d < 2) st_gran(ws, base + (size_t)(32 + d) * 8, tag, __float_as_uint(d == 0 ? M : L));
  }
  asm volatile("" ::: "memory");
  {
    const size_t base = WS_H2 + (size_t)role * 16 * H2S * 8;
    const int pp = 2 * w + (lane >= 34 ? 1 : 0);
    const int e0 = lane >= 34 ? lane - 34 : lane;
    unsigned v0 = 0, v1 = 0;
    for (unsigned spin = 0; spin < SPIN_CAP; ++spin) {
      bool ok = true;
      const u64 x0 = ld_gran(ws, base + (size_t)(pp * H2S + e0) * 8); v0 = (unsigned)x0; ok &= ((unsigned)(x0 >> 32) == mytag);
      if (lane < 4) { const u64 x1 = ld_gran(ws, base + (size_t)((2 * w + 1) * H2S + 30 + lane) * 8); v1 = (unsigned)x1; ok &= ((unsigned)(x1 >> 32) == mytag); }
      if (__all(ok)) break;
      __builtin_amdgcn_s_sleep(1);
    }
    h2l[pp][e0] = __uint_as_float(v0);
    if (lane < 4) h2l[2 * w + 1][30 + lane] = __uint_as_float(v1);
  }
  wg_barrier();
  if (tid < 128) {
    const int bb = tid >> 5, d = tid & 31;
    float M = -INFINITY;
#pragma unroll
    for (int ss = 0; ss < 4; ++ss) M = fmaxf(M, h2l[bb * 4 + ss][32]);
    float L = 0.f, o = 0.f;
#pragma unroll
    for (int ss = 0; ss < 4; ++ss) {
      const float wt = __builtin_amdgcn_exp2f((h2l[bb * 4 + ss][32] - M) * LOG2E);
      L = fmaf(wt, h2l[bb * 4 + ss][33], L); o = fmaf(wt, h2l[bb * 4 + ss][d], o);
    }
    ol[bb][d] = rbf(o / L);
  }
  wg_barrier();
  {
    float y[4][8];
#pragma unroll
    for (int bb = 0; bb < 4; ++bb)
#pragma unroll
      for (int k = 0; k < 8; ++k) y[bb][k] = 0.f;
#pragma unroll
    for (int i = 0; i < 4; ++i)
#pragma unroll
      for (int bb = 0; bb < 4; ++bb) {
        const float ov = ol[bb][4 * w + i];
#pragma unroll
        for (int k = 0; k < 4; ++k) { y[bb][2 * k] = fmaf(ov, bflo(Wr[i][k]), y[bb][2 * k]); y[bb][2 * k + 1] = fmaf(ov, bfhi(Wr[i][k]), y[bb][2 * k + 1]); }
      }
    float* yl = lds;
#pragma unroll
    for (int bb = 0; bb < 4; ++bb) {
      *reinterpret_cast<f32x4*>(&yl[(w * 4 + bb) * 512 + 8 * lane]) = f32x4{y[bb][0], y[bb][1], y[bb][2], y[bb][3]};
      *reinterpret_cast<f32x4*>(&yl[(w * 4 + bb) * 512 + 8 * lane + 4]) = f32x4{y[bb][4], y[bb][5], y[bb][6], y[bb][7]};
    }
  }
  wg_barrier();
  {
    const float* yl = lds;
#pragma unroll
    for (int i = 0; i < 4; ++i) {
      const int idx = tid + 512 * i;
      float sum = 0.f;
#pragma unroll
      for (int ww = 0; ww < 8; ++ww) sum += yl[ww * 2048 + idx];
      const int cc = idx >> 4, e = idx & 15;
      st_gran(ws, WS_H3 + (((size_t)(br * 128 + cc) * 128 + rem) * 16 + e) * 8, elds[cc] + 1u, __float_as_uint(sum));
    }
  }
  asm volatile("" ::: "memory");
  {
    const size_t base = WS_H3 + (size_t)role * 128 * 16 * 8;
    unsigned vals[4];
    for (unsigned spin = 0; spin < SPIN_CAP; ++spin) {
      bool ok = true;
#pragma unroll
      for (int k = 0; k < 4; ++k) { const u64 x = ld_gran(ws, base + (size_t)(tid + 512 * k) * 8); vals[k] = (unsigned)x; ok &= ((unsigned)(x >> 32) == mytag); }
      if (__all(ok)) break;
      __builtin_amdgcn_s_sleep(1);
    }
    const float sum = (__uint_as_float(vals[0]) + __uint_as_float(vals[1])) + (__uint_as_float(vals[2]) + __uint_as_float(vals[3]));
    fin[tid >> 4][tid & 15] = sum;
  }
  wg_barrier();
  if (tid < 16) {
    float t = 0.f;
#pragma unroll
    for (int i = 0; i < 32; ++i) t += fin[i][tid];
    const int idx = rem * 16 + tid, bb = idx >> 9, col = idx & 511;
    p.out[bb * 1024 + br * 512 + col] = f2bf(t);
  }
  if (tid == 0) __hip_atomic_store((gu32*)(ws + WS_E) + role, mytag, RLX_AGENT);
}

extern "C" void kernel_launch(void* const* d_in, const int* in_sizes, int n_in,
                              void* d_out, int out_size, void* d_ws, size_t ws_size,
                              hipStream_t stream) {
  Ptrs p;
  for (int br = 0; br < 2; ++br) {
    p.x[br] = (const unsigned short*)d_in[5 * br + 0];
    p.k[br] = (const unsigned short*)d_in[5 * br + 1];
    p.v[br] = (const unsigned short*)d_in[5 * br + 2];
    p.wq[br] = (const unsigned short*)d_in[5 * br + 3];
    p.wo[br] = (const unsigned short*)d_in[5 * br + 4];
  }
  p.out = (unsigned short*)d_out;
  p.ws = (unsigned char*)d_ws;
  hipLaunchKernelGGL(k_fused, dim3(256), dim3(512), 0, stream, p);
}
```

```cpp
#include <hip/hip_runtime.h>
#include <hip/hip_bf16.h>
#include <stdint.h>

typedef unsigned u32x4 __attribute__((ext_vector_type(4)));
typedef float f32x4 __attribute__((ext_vector_type(4)));
typedef __attribute__((ext_vector_type(2))) __bf16 bf16x2;
typedef unsigned long long u64;
#define GAS __attribute__((address_space(1)))
typedef GAS u64 gu64;
typedef GAS unsigned gu32;
#define RLX_AGENT __ATOMIC_RELAXED, __HIP_MEMORY_SCOPE_AGENT

constexpr int NB = 4, DM = 512, HQ = 32, HKV = 8, SK = 8192, DH = 128, WIN = 2048;
constexpr int NSPLIT = 4, KPS = WIN / NSPLIT;
constexpr float SCALE = 0.08838834764831845f;
constexpr float LOG2E = 1.4426950408889634f;
constexpr unsigned SPIN_CAP = 20000;

constexpr size_t WS_E = 0;
constexpr size_t WS_H1 = 4096;
constexpr size_t WS_H2 = WS_H1 + (size_t)256 * 256 * 8;
constexpr int H2S = 36;
constexpr size_t WS_H3 = WS_H2 + (size_t)256 * 16 * H2S * 8;
constexpr size_t WS_END = WS_H3 + (size_t)256 * 128 * 16 * 8;

__device__ __forceinline__ float bf2f(unsigned short h) { return __uint_as_float(((unsigned)h) << 16); }
__device__ __forceinline__ float bflo(unsigned u) { return __uint_as_float(u << 16); }
__device__ __forceinline__ float bfhi(unsigned u) { return __uint_as_float(u & 0xffff0000u); }
__device__ __forceinline__ float rbf(float f) { __bf16 h = (__bf16)f; return (float)h; }
__device__ __forceinline__ unsigned short f2bf(float f) { __bf16 h = (__bf16)f; return __builtin_bit_cast(unsigned short, h); }
__device__ __forceinline__ float dot2(unsigned k, unsigned q, float acc) {
  return __builtin_amdgcn_fdot2_f32_bf16(__builtin_bit_cast(bf16x2, k), __builtin_bit_cast(bf16x2, q), acc, false);
}
template <int CTRL> __device__ __forceinline__ float dpp(float x) {
  return __builtin_bit_cast(float, __builtin_amdgcn_mov_dpp(__builtin_bit_cast(int, x), CTRL, 0xf, 0xf, true));
}
constexpr int XOR1 = 0xB1, XOR2 = 0x4E, XOR7 = 0x141, XOR8 = 0x128, ROR4 = 0x124;
__device__ __forceinline__ float xrow16_max(float x) {
  auto s = __builtin_amdgcn_permlane16_swap(__float_as_uint(x), __float_as_uint(x), false, false);
  x = fmaxf(__uint_as_float(s[0]), __uint_as_float(s[1]));
  auto t = __builtin_amdgcn_permlane32_swap(__float_as_uint(x), __float_as_uint(x), false, false);
  return fmaxf(__uint_as_float(t[0]), __uint_as_float(t[1]));
}
__device__ __forceinline__ float xrow16_sum(float x) {
  auto s = __builtin_amdgcn_permlane16_swap(__float_as_uint(x), __float_as_uint(x), false, false);
  x = __uint_as_float(s[0]) + __uint_as_float(s[1]);
  auto t = __builtin_amdgcn_permlane32_swap(__float_as_uint(x), __float_as_uint(x), false, false);
  return __uint_as_float(t[0]) + __uint_as_float(t[1]);
}
__device__ __forceinline__ u32x4 ldnt(const void* p) { return __builtin_nontemporal_load(reinterpret_cast<const u32x4*>(p)); }
__device__ __forceinline__ void st_gran(unsigned char* ws, size_t byteoff, unsigned tag, unsigned val) {
  __hip_atomic_store((gu64*)(ws + byteoff), ((u64)tag << 32) | (u64)val, RLX_AGENT);
}
__device__ __forceinline__ u64 ld_gran(const unsigned char* ws, size_t byteoff) {
  return __hip_atomic_load((gu64*)(ws + byteoff), RLX_AGENT);
}
__device__ __forceinline__ void wg_barrier() {
  asm volatile("s_waitcnt lgkmcnt(0)" ::: "memory");
  __builtin_amdgcn_s_barrier();
  asm volatile("" ::: "memory");
}

#define SB() __builtin_amdgcn_sched_barrier(0)
#define STAMP(i) do {} while (0)
struct Ptrs {
  const unsigned short* x[2];
  const unsigned short* k[2];
  const unsigned short* v[2];
  const unsigned short* wq[2];
  const unsigned short* wo[2];
  unsigned short* out;
  unsigned char* ws;
};

__global__ void __launch_bounds__(512, 2) k_fused(Ptrs p) {
  __shared__ __attribute__((aligned(16))) float lds[8 * 4 * 512];
  __shared__ __attribute__((aligned(16))) unsigned qlds[256];
  __shared__ unsigned elds[128];
  __shared__ float wml[8][4][2];
  __shared__ float h2l[16][H2S];
  __shared__ float ol[4][32];
  __shared__ float fin[32][16];
  __shared__ unsigned qcnt;

  const int bid = blockIdx.x;
  const int role = (bid & 7) * 32 + (bid >> 3);
  const int br = role >> 7, rem = role & 127, g = rem >> 4, j = rem & 15, b = j >> 2, s = j & 3;
  const int tid = threadIdx.x, lane = tid & 63;
  const int w = __builtin_amdgcn_readfirstlane(tid >> 6);
  unsigned char* ws = p.ws;
  const int cbase = br * 128 + g * 16;

  STAMP(0);
  if (tid == 0) qcnt = 0u;
  const unsigned eword = __hip_atomic_load((gu32*)(ws + WS_E) + br * 128 + (tid & 127), RLX_AGENT);
  const int r = lane >> 4, c = lane & 15;
  const size_t kvbase = ((size_t)(b * HKV + g) * SK + (SK - WIN) + s * KPS + w * 64) * DH;
  const unsigned short* kp = p.k[br] + kvbase + (size_t)r * DH + 8 * c;
  const unsigned short* vp = p.v[br] + kvbase + (size_t)r * DH + 8 * c;
  u32x4 Kr[16], Vr[16];
  wg_barrier();
  unsigned mytag = 0;
  if (w < 4) {
    __builtin_amdgcn_s_setprio(3);
    const int qr = lane >> 2, qc = lane & 3;
    u32x4 W[8];
    {
      const unsigned short* wq = p.wq[br] + (size_t)(128 * w + qr) * 4096 + g * 512 + j * 32 + 8 * qc;
#pragma unroll
      for (int t = 0; t < 8; ++t) W[t] = ldnt(wq + (size_t)(16 * t) * 4096);
    }
    unsigned short xr[8][4];
    {
      const unsigned short* x = p.x[br] + 128 * w + qr;
#pragma unroll
      for (int t = 0; t < 8; ++t)
#pragma unroll
        for (int bb = 0; bb < 4; ++bb) xr[t][bb] = x[bb * DM + 16 * t];
    }
    if (tid < 128) elds[tid] = eword;
    float acc[4][8];
#pragma unroll
    for (int bb = 0; bb < 4; ++bb)
#pragma unroll
      for (int k = 0; k < 8; ++k) acc[bb][k] = 0.f;
#pragma unroll
    for (int t = 0; t < 8; ++t)
#pragma unroll
      for (int bb = 0; bb < 4; ++bb) {
        const float xv = bf2f(xr[t][bb]);
#pragma unroll
        for (int k = 0; k < 4; ++k) {
          acc[bb][2 * k] = fmaf(xv, bflo(W[t][k]), acc[bb][2 * k]);
          acc[bb][2 * k + 1] = fmaf(xv, bfhi(W[t][k]), acc[bb][2 * k + 1]);
        }
      }
#pragma unroll
    for (int bb = 0; bb < 4; ++bb)
#pragma unroll
      for (int k = 0; k < 8; ++k) {
        float v = acc[bb][k];
        v += dpp<ROR4>(v); v += dpp<XOR8>(v); v = xrow16_sum(v);
        acc[bb][k] = v;
      }
    if (qr == 0) {
      float* red = lds + w * 128;
#pragma unroll
      for (int bb = 0; bb < 4; ++bb)
#pragma unroll
        for (int k = 0; k < 8; ++k) red[bb * 32 + 8 * qc + k] = acc[bb][k];
    }
    asm volatile("s_waitcnt lgkmcnt(0)" ::: "memory");
    if (lane == 0) __hip_atomic_fetch_add(&qcnt, 1u, __ATOMIC_RELAXED, __HIP_MEMORY_SCOPE_WORKGROUP);
    STAMP(1);
  }
  const size_t h1base = WS_H1 + (size_t)role * 256 * 8;
  u64 g0 = 0, g1 = 0, g2 = 0, g3 = 0;
  if (w == 0) {
    for (unsigned spin = 0; spin < 1000000u; ++spin) {
      const unsigned cnt = __hip_atomic_load(&qcnt, __ATOMIC_RELAXED, __HIP_MEMORY_SCOPE_WORKGROUP);
      if (cnt >= 4u) break;
    }
    asm volatile("" ::: "memory");
#pragma unroll
    for (int k = 0; k < 4; ++k) {
      const int idx = k * 64 + lane, cc = idx >> 4, i = idx & 15, bb = cc >> 2;
      float s0 = 0.f, s1 = 0.f;
#pragma unroll
      for (int ww = 0; ww < 4; ++ww) { s0 += lds[ww * 128 + bb * 32 + 2 * i]; s1 += lds[ww * 128 + bb * 32 + 2 * i + 1]; }
      const unsigned val = (unsigned)f2bf(s0) | ((unsigned)f2bf(s1) << 16);
      st_gran(ws, WS_H1 + ((size_t)(cbase + cc) * 256 + j * 16 + i) * 8, elds[g * 16 + cc] + 1u, val);
    }
    STAMP(2);
    g0 = ld_gran(ws, h1base + (size_t)(0 * 64 + lane) * 8);
    g1 = ld_gran(ws, h1base + (size_t)(1 * 64 + lane) * 8);
    g2 = ld_gran(ws, h1base + (size_t)(2 * 64 + lane) * 8);
    g3 = ld_gran(ws, h1base + (size_t)(3 * 64 + lane) * 8);
  }
  SB();
  if (w != 0) __builtin_amdgcn_s_setprio(0);
#pragma unroll
  for (int t = 0; t < 16; ++t) { Kr[t] = ldnt(kp + (size_t)t * 4 * DH); SB(); }
  if (w == 0) {
    mytag = elds[g * 16 + j] + 1u;
    const bool ok0 = ((unsigned)(g0 >> 32) == mytag) & ((unsigned)(g1 >> 32) == mytag) & ((unsigned)(g2 >> 32) == mytag) & ((unsigned)(g3 >> 32) == mytag);
    if (!__all(ok0)) {
      for (unsigned spin = 0; spin < SPIN_CAP; ++spin) {
        g0 = ld_gran(ws, h1base + (size_t)(0 * 64 + lane) * 8);
        g1 = ld_gran(ws, h1base + (size_t)(1 * 64 + lane) * 8);
        g2 = ld_gran(ws, h1base + (size_t)(2 * 64 + lane) * 8);
        g3 = ld_gran(ws, h1base + (size_t)(3 * 64 + lane) * 8);
        const bool ok = ((unsigned)(g0 >> 32) == mytag) & ((unsigned)(g1 >> 32) == mytag) & ((unsigned)(g2 >> 32) == mytag) & ((unsigned)(g3 >> 32) == mytag);
        if (__all(ok)) break;
        __builtin_amdgcn_s_sleep(1);
      }
    }
    qlds[0 * 64 + lane] = (unsigned)g0; qlds[1 * 64 + lane] = (unsigned)g1; qlds[2 * 64 + lane] = (unsigned)g2; qlds[3 * 64 + lane] = (unsigned)g3;
  }
  __builtin_amdgcn_s_setprio(0);
  STAMP(3);
  wg_barrier();
  STAMP(4);
  mytag = elds[g * 16 + j] + 1u;
  u32x4 qp[4];
#pragma unroll
  for (int h = 0; h < 4; ++h) qp[h] = *reinterpret_cast<const u32x4*>(&qlds[h * 64 + 4 * c]);
  float sc[4];
#pragma unroll
  for (int gi = 0; gi < 4; ++gi) {
    float x[16];
#pragma unroll
    for (int tl = 0; tl < 4; ++tl)
#pragma unroll
      for (int h = 0; h < 4; ++h) {
        float d = 0.f;
#pragma unroll
        for (int k = 0; k < 4; ++k) d = dot2(Kr[4 * gi + tl][k], qp[h][k], d);
        x[tl * 4 + h] = d;
      }
    SB();
#pragma unroll
    for (int tl = 0; tl < 4; ++tl) { Vr[4 * gi + tl] = ldnt(vp + (size_t)(4 * gi + tl) * 4 * DH); SB(); }
#define TR_STEP(HALF, CTRL, BIT) _Pragma("unroll") for (int hh = 0; hh < (HALF); ++hh) { \
      const float lo_ = x[hh], hi_ = x[hh + (HALF)]; const float t1_ = lo_ + dpp<CTRL>(lo_), t2_ = hi_ + dpp<CTRL>(hi_); \
      x[hh] = (c & (BIT)) ? t2_ : t1_; }
    TR_STEP(8, XOR8, 8) TR_STEP(4, XOR7, 4) TR_STEP(2, XOR2, 2) TR_STEP(1, XOR1, 1)
#undef TR_STEP
    sc[gi] = rbf(rbf(x[0]) * SCALE);
  }
  u32x4 Wr[4];
  {
    const unsigned short* wo = p.wo[br] + (size_t)(g * 512 + j * 32 + (lane >> 3)) * DM + 64 * w + 8 * (lane & 7);
    SB();
#pragma unroll
    for (int i = 0; i < 4; ++i) Wr[i] = ldnt(wo + (size_t)(8 * i) * DM);
    SB();
  }
  STAMP(5);
  float m = fmaxf(fmaxf(sc[0], sc[1]), fmaxf(sc[2], sc[3]));
  m = fmaxf(m, dpp<ROR4>(m)); m = fmaxf(m, dpp<XOR8>(m)); m = xrow16_max(m);
  float* pl = lds + w * 256;
  float l = 0.f;
#pragma unroll
  for (int gi = 0; gi < 4; ++gi) {
    const float pv = __builtin_amdgcn_exp2f((sc[gi] - m) * LOG2E);
    l += pv;
    pl[(4 * (4 * gi + (c >> 2)) + r) * 4 + (c & 3)] = pv;
  }
  l += dpp<ROR4>(l); l += dpp<XOR8>(l); l = xrow16_sum(l);
  if (lane < 4) { wml[w][lane][0] = m; wml[w][lane][1] = l; }
  asm volatile("s_waitcnt lgkmcnt(0)" ::: "memory");
  __builtin_amdgcn_wave_barrier();
  float acc[4][8];
#pragma unroll
  for (int h = 0; h < 4; ++h)
#pragma unroll
    for (int k = 0; k < 8; ++k) acc[h][k] = 0.f;
#pragma unroll
  for (int t = 0; t < 16; ++t) {
    const f32x4 pr = *reinterpret_cast<const f32x4*>(&pl[(4 * t + r) * 4]);
    float vf[8];
#pragma unroll
    for (int k = 0; k < 4; ++k) { vf[2 * k] = bflo(Vr[t][k]); vf[2 * k + 1] = bfhi(Vr[t][k]); }
#pragma unroll
    for (int h = 0; h < 4; ++h)
#pragma unroll
      for (int k = 0; k < 8; ++k) acc[h][k] = fmaf(pr[h], vf[k], acc[h][k]);
  }
#pragma unroll
  for (int h = 0; h < 4; ++h)
#pragma unroll
    for (int k = 0; k < 8; ++k) acc[h][k] = xrow16_sum(acc[h][k]);
  STAMP(6);
  float* accl = lds + 2048;
  if (r == 0) {
#pragma unroll
    for (int h = 0; h < 4; ++h) {
      *reinterpret_cast<f32x4*>(&accl[(w * 4 + h) * 128 + 8 * c]) = f32x4{acc[h][0], acc[h][1], acc[h][2], acc[h][3]};
      *reinterpret_cast<f32x4*>(&accl[(w * 4 + h) * 128 + 8 * c + 4]) = f32x4{acc[h][4], acc[h][5], acc[h][6], acc[h][7]};
    }
  }
  wg_barrier();
  STAMP(7);
  {
    const int jc = tid >> 5, d = tid & 31, h = jc >> 2, dq = jc & 3;
    float M = wml[0][h][0];
#pragma unroll
    for (int ww = 1; ww < 8; ++ww) M = fmaxf(M, wml[ww][h][0]);
    float o = 0.f, L = 0.f;
#pragma unroll
    for (int ww = 0; ww < 8; ++ww) {
      const float wt = __builtin_amdgcn_exp2f((wml[ww][h][0] - M) * LOG2E);
      o = fmaf(wt, accl[(ww * 4 + h) * 128 + dq * 32 + d], o);
      L = fmaf(wt, wml[ww][h][1], L);
    }
    const unsigned tag = elds[g * 16 + jc] + 1u;
    const size_t base = WS_H2 + ((size_t)(cbase + jc) * 16 + j) * H2S * 8;
    st_gran(ws, base + (size_t)d * 8, tag, __float_as_uint(o));
    if (d < 2) st_gran(ws, base + (size_t)(32 + d) * 8, tag, __float_as_uint(d == 0 ? M : L));
  }
  asm volatile("" ::: "memory");
  STAMP(8);
  {
    const size_t base = WS_H2 + (size_t)role * 16 * H2S * 8;
    const int pp = 2 * w + (lane >= 34 ? 1 : 0);
    const int e0 = lane >= 34 ? lane - 34 : lane;
    unsigned v0 = 0, v1 = 0;
    for (unsigned spin = 0; spin < SPIN_CAP; ++spin) {
      bool ok = true;
      const u64 x0 = ld_gran(ws, base + (size_t)(pp * H2S + e0) * 8); v0 = (unsigned)x0; ok &= ((unsigned)(x0 >> 32) == mytag);
      if (lane < 4) { const u64 x1 = ld_gran(ws, base + (size_t)((2 * w + 1) * H2S + 30 + lane) * 8); v1 = (unsigned)x1; ok &= ((unsigned)(x1 >> 32) == mytag); }
      if (__all(ok)) break;
      __builtin_amdgcn_s_sleep(1);
    }
    h2l[pp][e0] = __uint_as_float(v0);
    if (lane < 4) h2l[2 * w + 1][30 + lane] = __uint_as_float(v1);
  }
  STAMP(9);
  wg_barrier();
  {
    const int rr = lane >> 3;
    float* ow = lds + 2048 + w * 128;
    {
      const int bb = lane >> 4, d = 2 * (lane & 15);
      const float m0 = h2l[bb * 4 + 0][32], m1 = h2l[bb * 4 + 1][32], m2 = h2l[bb * 4 + 2][32], m3 = h2l[bb * 4 + 3][32];
      const float M = fmaxf(fmaxf(m0, m1), fmaxf(m2, m3));
      const float w0 = __builtin_amdgcn_exp2f((m0 - M) * LOG2E), w1 = __builtin_amdgcn_exp2f((m1 - M) * LOG2E);
      const float w2 = __builtin_amdgcn_exp2f((m2 - M) * LOG2E), w3 = __builtin_amdgcn_exp2f((m3 - M) * LOG2E);
      const float L = fmaf(w0, h2l[bb * 4 + 0][33], fmaf(w1, h2l[bb * 4 + 1][33], fmaf(w2, h2l[bb * 4 + 2][33], w3 * h2l[bb * 4 + 3][33])));
      const float o0 = fmaf(w0, h2l[bb * 4 + 0][d], fmaf(w1, h2l[bb * 4 + 1][d], fmaf(w2, h2l[bb * 4 + 2][d], w3 * h2l[bb * 4 + 3][d])));
      const float o1 = fmaf(w0, h2l[bb * 4 + 0][d + 1], fmaf(w1, h2l[bb * 4 + 1][d + 1], fmaf(w2, h2l[bb * 4 + 2][d + 1], w3 * h2l[bb * 4 + 3][d + 1])));
      ow[bb * 32 + d] = rbf(o0 / L);
      ow[bb * 32 + d + 1] = rbf(o1 / L);
    }
    asm volatile("s_waitcnt lgkmcnt(0)" ::: "memory");
    __builtin_amdgcn_wave_barrier();
    float y[4][8];
#pragma unroll
    for (int bb = 0; bb < 4; ++bb)
#pragma unroll
      for (int k = 0; k < 8; ++k) y[bb][k] = 0.f;
#pragma unroll
    for (int i = 0; i < 4; ++i)
#pragma unroll
      for (int bb = 0; bb < 4; ++bb) {
        const float ov = ow[bb * 32 + 8 * i + rr];
#pragma unroll
        for (int k = 0; k < 4; ++k) { y[bb][2 * k] = fmaf(ov, bflo(Wr[i][k]), y[bb][2 * k]); y[bb][2 * k + 1] = fmaf(ov, bfhi(Wr[i][k]), y[bb][2 * k + 1]); }
      }
#pragma unroll
    for (int bb = 0; bb < 4; ++bb)
#pragma unroll
      for (int k = 0; k < 8; ++k) { float v = y[bb][k]; v += dpp<XOR8>(v); y[bb][k] = xrow16_sum(v); }
    {
      float* yx = lds + w * 256;
      const int cc = lane & 7;
      if (rr == 0) {
#pragma unroll
        for (int bb = 0; bb < 4; ++bb) {
          *reinterpret_cast<f32x4*>(&yx[bb * 64 + 8 * cc]) = f32x4{y[bb][0], y[bb][1], y[bb][2], y[bb][3]};
          *reinterpret_cast<f32x4*>(&yx[bb * 64 + 8 * cc + 4]) = f32x4{y[bb][4], y[bb][5], y[bb][6], y[bb][7]};
        }
      }
      asm volatile("s_waitcnt lgkmcnt(0)" ::: "memory");
      __builtin_amdgcn_wave_barrier();
#pragma unroll
      for (int bb = 0; bb < 4; ++bb) {
        const float v = yx[bb * 64 + lane];
        const int idx = bb * 512 + 64 * w + lane;
        const int cns = idx >> 4, e = idx & 15;
        st_gran(ws, WS_H3 + (((size_t)(br * 128 + cns) * 128 + rem) * 16 + e) * 8, elds[cns] + 1u, __float_as_uint(v));
      }
    }
  }
  asm volatile("" ::: "memory");
  STAMP(10);
  {
    const size_t base = WS_H3 + (size_t)role * 128 * 16 * 8;
    unsigned vals[4];
    for (unsigned spin = 0; spin < SPIN_CAP; ++spin) {
      bool ok = true;
#pragma unroll
      for (int k = 0; k < 4; ++k) { const u64 x = ld_gran(ws, base + (size_t)(tid + 512 * k) * 8); vals[k] = (unsigned)x; ok &= ((unsigned)(x >> 32) == mytag); }
      if (__all(ok)) break;
      __builtin_amdgcn_s_sleep(1);
    }
    const float sum = (__uint_as_float(vals[0]) + __uint_as_float(vals[1])) + (__uint_as_float(vals[2]) + __uint_as_float(vals[3]));
    fin[tid >> 4][tid & 15] = sum;
  }
  STAMP(11);
  wg_barrier();
  if (tid < 16) {
    float t = 0.f;
#pragma unroll
    for (int i = 0; i < 32; ++i) t += fin[i][tid];
    const int idx = rem * 16 + tid, bb = idx >> 9, col = idx & 511;
    p.out[bb * 1024 + br * 512 + col] = f2bf(t);
  }
  if (tid == 0) __hip_atomic_store((gu32*)(ws + WS_E) + role, mytag, RLX_AGENT);
  STAMP(12);
}

extern "C" void kernel_launch(void* const* d_in, const int* in_sizes, int n_in,
                              void* d_out, int out_size, void* d_ws, size_t ws_size,
                              hipStream_t stream) {
  Ptrs p;
  for (int br = 0; br < 2; ++br) {
    p.x[br] = (const unsigned short*)d_in[5 * br + 0];
    p.k[br] = (const unsigned short*)d_in[5 * br + 1];
    p.v[br] = (const unsigned short*)d_in[5 * br + 2];
    p.wq[br] = (const unsigned short*)d_in[5 * br + 3];
    p.wo[br] = (const unsigned short*)d_in[5 * br + 4];
  }
  p.out = (unsigned short*)d_out;
  p.ws = (unsigned char*)d_ws;
  hipLaunchKernelGGL(k_fused, dim3(256), dim3(512), 0, stream, p);
}
```

```cpp
#include <hip/hip_runtime.h>
#include <hip/hip_bf16.h>
#include <stdint.h>

typedef unsigned u32x4 __attribute__((ext_vector_type(4)));
typedef float f32x4 __attribute__((ext_vector_type(4)));
typedef __attribute__((ext_vector_type(2))) __bf16 bf16x2;
typedef unsigned long long u64;
#define GAS __attribute__((address_space(1)))
typedef GAS u64 gu64;
typedef GAS unsigned gu32;
#define RLX_AGENT __ATOMIC_RELAXED, __HIP_MEMORY_SCOPE_AGENT

constexpr int NB = 4, DM = 512, HQ = 32, HKV = 8, SK = 8192, DH = 128, WIN = 2048;
constexpr int NSPLIT = 4, KPS = WIN / NSPLIT;
constexpr float SCALE = 0.08838834764831845f;
constexpr float LOG2E = 1.4426950408889634f;
constexpr unsigned SPIN_CAP = 20000;

constexpr size_t WS_E = 0;
constexpr size_t WS_H1 = 4096;
constexpr size_t WS_H2 = WS_H1 + (size_t)256 * 256 * 8;
constexpr int H2S = 36;
constexpr size_t WS_H3 = WS_H2 + (size_t)256 * 16 * H2S * 8;
constexpr size_t WS_END = WS_H3 + (size_t)256 * 128 * 16 * 8;

__device__ __forceinline__ float bf2f(unsigned short h) { return __uint_as_float(((unsigned)h) << 16); }
__device__ __forceinline__ float bflo(unsigned u) { return __uint_as_float(u << 16); }
__device__ __forceinline__ float bfhi(unsigned u) { return __uint_as_float(u & 0xffff0000u); }
__device__ __forceinline__ float rbf(float f) { __bf16 h = (__bf16)f; return (float)h; }
__device__ __forceinline__ unsigned short f2bf(float f) { __bf16 h = (__bf16)f; return __builtin_bit_cast(unsigned short, h); }
__device__ __forceinline__ float dot2(unsigned k, unsigned q, float acc) {
  return __builtin_amdgcn_fdot2_f32_bf16(__builtin_bit_cast(bf16x2, k), __builtin_bit_cast(bf16x2, q), acc, false);
}
template <int CTRL> __device__ __forceinline__ float dpp(float x) {
  return __builtin_bit_cast(float, __builtin_amdgcn_mov_dpp(__builtin_bit_cast(int, x), CTRL, 0xf, 0xf, true));
}
constexpr int XOR1 = 0xB1, XOR2 = 0x4E, XOR7 = 0x141, XOR8 = 0x128, ROR4 = 0x124;
__device__ __forceinline__ float xrow16_max(float x) {
  auto s = __builtin_amdgcn_permlane16_swap(__float_as_uint(x), __float_as_uint(x), false, false);
  x = fmaxf(__uint_as_float(s[0]), __uint_as_float(s[1]));
  auto t = __builtin_amdgcn_permlane32_swap(__float_as_uint(x), __float_as_uint(x), false, false);
  return fmaxf(__uint_as_float(t[0]), __uint_as_float(t[1]));
}
__device__ __forceinline__ float xrow16_sum(float x) {
  auto s = __builtin_amdgcn_permlane16_swap(__float_as_uint(x), __float_as_uint(x), false, false);
  x = __uint_as_float(s[0]) + __uint_as_float(s[1]);
  auto t = __builtin_amdgcn_permlane32_swap(__float_as_uint(x), __float_as_uint(x), false, false);
  return __uint_as_float(t[0]) + __uint_as_float(t[1]);
}
__device__ __forceinline__ float tr16(float a, float b) {
  auto s = __builtin_amdgcn_permlane16_swap(__float_as_uint(a), __float_as_uint(b), false, false);
  return __uint_as_float(s[0]) + __uint_as_float(s[1]);
}
__device__ __forceinline__ float tr32(float a, float b) {
  auto s = __builtin_amdgcn_permlane32_swap(__float_as_uint(a), __float_as_uint(b), false, false);
  return __uint_as_float(s[0]) + __uint_as_float(s[1]);
}
__device__ __forceinline__ u32x4 ldnt(const void* p) { return __builtin_nontemporal_load(reinterpret_cast<const u32x4*>(p)); }
__device__ __forceinline__ void st_gran(unsigned char* ws, size_t byteoff, unsigned tag, unsigned val) {
  __hip_atomic_store((gu64*)(ws + byteoff), ((u64)tag << 32) | (u64)val, RLX_AGENT);
}
__device__ __forceinline__ u64 ld_gran(const unsigned char* ws, size_t byteoff) {
  return __hip_atomic_load((gu64*)(ws + byteoff), RLX_AGENT);
}
__device__ __forceinline__ void wg_barrier() {
  asm volatile("s_waitcnt lgkmcnt(0)" ::: "memory");
  __builtin_amdgcn_s_barrier();
  asm volatile("" ::: "memory");
}

#define SB() __builtin_amdgcn_sched_barrier(0)
#define STAMP(i) do {} while (0)
struct Ptrs {
  const unsigned short* x[2];
  const unsigned short* k[2];
  const unsigned short* v[2];
  const unsigned short* wq[2];
  const unsigned short* wo[2];
  unsigned short* out;
  unsigned char* ws;
};

__global__ void __launch_bounds__(512, 2) k_fused(Ptrs p) {
  __shared__ __attribute__((aligned(16))) float lds[8 * 4 * 512];
  __shared__ __attribute__((aligned(16))) unsigned qlds[256];
  __shared__ unsigned elds[128];
  __shared__ float wml[8][4][2];
  __shared__ float h2l[16][H2S];
  __shared__ float ol[4][32];
  __shared__ float fin[64][16];
  __shared__ unsigned qcnt;

  const int bid = blockIdx.x;
  const int role = (bid & 7) * 32 + (bid >> 3);
  const int br = role >> 7, rem = role & 127, g = rem >> 4, j = rem & 15, b = j >> 2, s = j & 3;
  const int tid = threadIdx.x, lane = tid & 63;
  const int w = __builtin_amdgcn_readfirstlane(tid >> 6);
  unsigned char* ws = p.ws;
  const int cbase = br * 128 + g * 16;
  const __amdgpu_buffer_rsrc_t rsrc = __builtin_amdgcn_make_buffer_rsrc(ws, 0, (int)WS_END, 0x00020000);

  STAMP(0);
  if (tid == 0) qcnt = 0u;
  const unsigned eword = __hip_atomic_load((gu32*)(ws + WS_E) + br * 128 + (tid & 127), RLX_AGENT);
  const int r = lane >> 4, c = lane & 15;
  const size_t kvbase = ((size_t)(b * HKV + g) * SK + (SK - WIN) + s * KPS + w * 64) * DH;
  const unsigned short* kp = p.k[br] + kvbase + (size_t)r * DH + 8 * c;
  const unsigned short* vp = p.v[br] + kvbase + (size_t)r * DH + 8 * c;
  u32x4 Kr[16], Vr[16];
  wg_barrier();
  unsigned mytag = 0;
  if (w < 4) {
    __builtin_amdgcn_s_setprio(3);
    const int qr = lane >> 2, qc = lane & 3;
    u32x4 W[8];
    {
      const unsigned short* wq = p.wq[br] + (size_t)(128 * w + qr) * 4096 + g * 512 + j * 32 + 8 * qc;
#pragma unroll
      for (int t = 0; t < 8; ++t) W[t] = ldnt(wq + (size_t)(16 * t) * 4096);
    }
    unsigned short xr[8][4];
    {
      const unsigned short* x = p.x[br] + 128 * w + qr;
#pragma unroll
      for (int t = 0; t < 8; ++t)
#pragma unroll
        for (int bb = 0; bb < 4; ++bb) xr[t][bb] = x[bb * DM + 16 * t];
    }
    if (tid < 128) elds[tid] = eword;
    float acc[4][8];
#pragma unroll
    for (int bb = 0; bb < 4; ++bb)
#pragma unroll
      for (int k = 0; k < 8; ++k) acc[bb][k] = 0.f;
#pragma unroll
    for (int t = 0; t < 8; ++t)
#pragma unroll
      for (int bb = 0; bb < 4; ++bb) {
        const float xv = bf2f(xr[t][bb]);
#pragma unroll
        for (int k = 0; k < 4; ++k) {
          acc[bb][2 * k] = fmaf(xv, bflo(W[t][k]), acc[bb][2 * k]);
          acc[bb][2 * k + 1] = fmaf(xv, bfhi(W[t][k]), acc[bb][2 * k + 1]);
        }
      }
#pragma unroll
    for (int bb = 0; bb < 4; ++bb)
#pragma unroll
      for (int k = 0; k < 8; ++k) {
        float v = acc[bb][k];
        v += dpp<ROR4>(v); v += dpp<XOR8>(v); v = xrow16_sum(v);
        acc[bb][k] = v;
      }
    if (qr == 0) {
      float* red = lds + w * 128;
#pragma unroll
      for (int bb = 0; bb < 4; ++bb)
#pragma unroll
        for (int k = 0; k < 8; ++k) red[bb * 32 + 8 * qc + k] = acc[bb][k];
    }
    asm volatile("s_waitcnt lgkmcnt(0)" ::: "memory");
    if (lane == 0) __hip_atomic_fetch_add(&qcnt, 1u, __ATOMIC_RELAXED, __HIP_MEMORY_SCOPE_WORKGROUP);
    STAMP(1);
  }
  const size_t h1base = WS_H1 + (size_t)role * 256 * 8;
  u64 g0 = 0, g1 = 0, g2 = 0, g3 = 0;
  if (w == 0) {
    for (unsigned spin = 0; spin < 1000000u; ++spin) {
      const unsigned cnt = __hip_atomic_load(&qcnt, __ATOMIC_RELAXED, __HIP_MEMORY_SCOPE_WORKGROUP);
      if (cnt >= 4u) break;
    }
    asm volatile("" ::: "memory");
#pragma unroll
    for (int k = 0; k < 4; ++k) {
      const int idx = k * 64 + lane, cc = idx >> 4, i = idx & 15, bb = cc >> 2;
      float s0 = 0.f, s1 = 0.f;
#pragma unroll
      for (int ww = 0; ww < 4; ++ww) { s0 += lds[ww * 128 + bb * 32 + 2 * i]; s1 += lds[ww * 128 + bb * 32 + 2 * i + 1]; }
      const unsigned val = (unsigned)f2bf(s0) | ((unsigned)f2bf(s1) << 16);
      st_gran(ws, WS_H1 + ((size_t)(cbase + cc) * 256 + j * 16 + i) * 8, elds[g * 16 + cc] + 1u, val);
    }
    STAMP(2);
    g0 = ld_gran(ws, h1base + (size_t)(0 * 64 + lane) * 8);
    g1 = ld_gran(ws, h1base + (size_t)(1 * 64 + lane) * 8);
    g2 = ld_gran(ws, h1base + (size_t)(2 * 64 + lane) * 8);
    g3 = ld_gran(ws, h1base + (size_t)(3 * 64 + lane) * 8);
  }
  SB();
  if (w != 0) __builtin_amdgcn_s_setprio(0);
#pragma unroll
  for (int t = 0; t < 16; ++t) { Kr[t] = ldnt(kp + (size_t)t * 4 * DH); SB(); }
  if (w == 0) {
    mytag = elds[g * 16 + j] + 1u;
    const bool ok0 = ((unsigned)(g0 >> 32) == mytag) & ((unsigned)(g1 >> 32) == mytag) & ((unsigned)(g2 >> 32) == mytag) & ((unsigned)(g3 >> 32) == mytag);
    if (!__all(ok0)) {
      for (unsigned spin = 0; spin < SPIN_CAP; ++spin) {
        g0 = ld_gran(ws, h1base + (size_t)(0 * 64 + lane) * 8);
        g1 = ld_gran(ws, h1base + (size_t)(1 * 64 + lane) * 8);
        g2 = ld_gran(ws, h1base + (size_t)(2 * 64 + lane) * 8);
        g3 = ld_gran(ws, h1base + (size_t)(3 * 64 + lane) * 8);
        const bool ok = ((unsigned)(g0 >> 32) == mytag) & ((unsigned)(g1 >> 32) == mytag) & ((unsigned)(g2 >> 32) == mytag) & ((unsigned)(g3 >> 32) == mytag);
        if (__all(ok)) break;
        __builtin_amdgcn_s_sleep(1);
      }
    }
    qlds[0 * 64 + lane] = (unsigned)g0; qlds[1 * 64 + lane] = (unsigned)g1; qlds[2 * 64 + lane] = (unsigned)g2; qlds[3 * 64 + lane] = (unsigned)g3;
  }
  __builtin_amdgcn_s_setprio(0);
  STAMP(3);
  wg_barrier();
  STAMP(4);
  mytag = elds[g * 16 + j] + 1u;
  u32x4 qp[4];
#pragma unroll
  for (int h = 0; h < 4; ++h) qp[h] = *reinterpret_cast<const u32x4*>(&qlds[h * 64 + 4 * c]);
  float sc[4];
#pragma unroll
  for (int gi = 0; gi < 4; ++gi) {
    float x[16];
#pragma unroll
    for (int tl = 0; tl < 4; ++tl)
#pragma unroll
      for (int h = 0; h < 4; ++h) {
        float d = 0.f;
#pragma unroll
        for (int k = 0; k < 4; ++k) d = dot2(Kr[4 * gi + tl][k], qp[h][k], d);
        x[tl * 4 + h] = d;
      }
    SB();
#pragma unroll
    for (int tl = 0; tl < 4; ++tl) { Vr[4 * gi + tl] = ldnt(vp + (size_t)(4 * gi + tl) * 4 * DH); SB(); }
#define TR_STEP(HALF, CTRL, BIT) _Pragma("unroll") for (int hh = 0; hh < (HALF); ++hh) { \
      const float lo_ = x[hh], hi_ = x[hh + (HALF)]; const float t1_ = lo_ + dpp<CTRL>(lo_), t2_ = hi_ + dpp<CTRL>(hi_); \
      x[hh] = (c & (BIT)) ? t2_ : t1_; }
    TR_STEP(8, XOR8, 8) TR_STEP(4, XOR7, 4) TR_STEP(2, XOR2, 2) TR_STEP(1, XOR1, 1)
#undef TR_STEP
    sc[gi] = rbf(rbf(x[0]) * SCALE);
  }
  u32x4 Wr[4];
  {
    const unsigned short* wo = p.wo[br] + (size_t)(g * 512 + j * 32 + (lane >> 3)) * DM + 64 * w + 8 * (lane & 7);
    SB();
#pragma unroll
    for (int i = 0; i < 4; ++i) Wr[i] = ldnt(wo + (size_t)(8 * i) * DM);
    SB();
  }
  STAMP(5);
  float m = fmaxf(fmaxf(sc[0], sc[1]), fmaxf(sc[2], sc[3]));
  m = fmaxf(m, dpp<ROR4>(m)); m = fmaxf(m, dpp<XOR8>(m)); m = xrow16_max(m);
  unsigned short* pl = reinterpret_cast<unsigned short*>(lds + w * 256);
  float l = 0.f;
#pragma unroll
  for (int gi = 0; gi < 4; ++gi) {
    const float pv = __builtin_amdgcn_exp2f((sc[gi] - m) * LOG2E);
    l += pv;
    const int t = 4 * gi + (c >> 2);
    pl[(((t >> 1) * 4 + r) * 4 + (c & 3)) * 2 + (t & 1)] = f2bf(pv);
  }
  l += dpp<ROR4>(l); l += dpp<XOR8>(l); l = xrow16_sum(l);
  if (lane < 4) { wml[w][lane][0] = m; wml[w][lane][1] = l; }
  asm volatile("s_waitcnt lgkmcnt(0)" ::: "memory");
  __builtin_amdgcn_wave_barrier();
  float acc[32];
#pragma unroll
  for (int k = 0; k < 32; ++k) acc[k] = 0.f;
#pragma unroll
  for (int u = 0; u < 8; ++u) {
    const u32x4 pr = *reinterpret_cast<const u32x4*>(&pl[((u * 4 + r) * 4) * 2]);
    unsigned vv[8];
#pragma unroll
    for (int k = 0; k < 4; ++k) {
      vv[2 * k] = __builtin_amdgcn_perm(Vr[2 * u + 1][k], Vr[2 * u][k], 0x05040100u);
      vv[2 * k + 1] = __builtin_amdgcn_perm(Vr[2 * u + 1][k], Vr[2 * u][k], 0x07060302u);
    }
#pragma unroll
    for (int h = 0; h < 4; ++h)
#pragma unroll
      for (int k = 0; k < 8; ++k) acc[h * 8 + k] = dot2(vv[k], pr[h], acc[h * 8 + k]);
  }
#pragma unroll
  for (int i = 0; i < 16; ++i) acc[i] = tr16(acc[i], acc[i + 16]);
#pragma unroll
  for (int i = 0; i < 8; ++i) acc[i] = tr32(acc[i], acc[i + 8]);
  STAMP(6);
  float* accl = lds + 2048;
  {
    const int hh = 2 * (r & 1) + (r >> 1);
    *reinterpret_cast<f32x4*>(&accl[(w * 4 + hh) * 128 + 8 * c]) = f32x4{acc[0], acc[1], acc[2], acc[3]};
    *reinterpret_cast<f32x4*>(&accl[(w * 4 + hh) * 128 + 8 * c + 4]) = f32x4{acc[4], acc[5], acc[6], acc[7]};
  }
  wg_barrier();
  STAMP(7);
  {
    const int jc = tid >> 5, d = tid & 31, h = jc >> 2, dq = jc & 3;
    float M = wml[0][h][0];
#pragma unroll
    for (int ww = 1; ww < 8; ++ww) M = fmaxf(M, wml[ww][h][0]);
    float o = 0.f, L = 0.f;
#pragma unroll
    for (int ww = 0; ww < 8; ++ww) {
      const float wt = __builtin_amdgcn_exp2f((wml[ww][h][0] - M) * LOG2E);
      o = fmaf(wt, accl[(ww * 4 + h) * 128 + dq * 32 + d], o);
      L = fmaf(wt, wml[ww][h][1], L);
    }
    const unsigned tag = elds[g * 16 + jc] + 1u;
    const size_t base = WS_H2 + ((size_t)(cbase + jc) * 16 + j) * H2S * 8;
    st_gran(ws, base + (size_t)d * 8, tag, __float_as_uint(o));
    if (d < 2) st_gran(ws, base + (size_t)(32 + d) * 8, tag, __float_as_uint(d == 0 ? M : L));
  }
  asm volatile("" ::: "memory");
  STAMP(8);
  {
    const size_t base = WS_H2 + (size_t)role * 16 * H2S * 8;
    const int pp = 2 * w + (lane >= 34 ? 1 : 0);
    const int e0 = lane >= 34 ? lane - 34 : lane;
    unsigned v0 = 0, v1 = 0;
    for (unsigned spin = 0; spin < SPIN_CAP; ++spin) {
      bool ok = true;
      const u64 x0 = ld_gran(ws, base + (size_t)(pp * H2S + e0) * 8); v0 = (unsigned)x0; ok &= ((unsigned)(x0 >> 32) == mytag);
      if (lane < 4) { const u64 x1 = ld_gran(ws, base + (size_t)((2 * w + 1) * H2S + 30 + lane) * 8); v1 = (unsigned)x1; ok &= ((unsigned)(x1 >> 32) == mytag); }
      if (__all(ok)) break;
      __builtin_amdgcn_s_sleep(1);
    }
    h2l[pp][e0] = __uint_as_float(v0);
    if (lane < 4) h2l[2 * w + 1][30 + lane] = __uint_as_float(v1);
  }
  STAMP(9);
  wg_barrier();
  {
    const int rr = lane >> 3;
    float* ow = lds + 2048 + w * 128;
    {
      const int bb = lane >> 4, d = 2 * (lane & 15);
      const float m0 = h2l[bb * 4 + 0][32], m1 = h2l[bb * 4 + 1][32], m2 = h2l[bb * 4 + 2][32], m3 = h2l[bb * 4 + 3][32];
      const float M = fmaxf(fmaxf(m0, m1), fmaxf(m2, m3));
      const float w0 = __builtin_amdgcn_exp2f((m0 - M) * LOG2E), w1 = __builtin_amdgcn_exp2f((m1 - M) * LOG2E);
      const float w2 = __builtin_amdgcn_exp2f((m2 - M) * LOG2E), w3 = __builtin_amdgcn_exp2f((m3 - M) * LOG2E);
      const float L = fmaf(w0, h2l[bb * 4 + 0][33], fmaf(w1, h2l[bb * 4 + 1][33], fmaf(w2, h2l[bb * 4 + 2][33], w3 * h2l[bb * 4 + 3][33])));
      const float o0 = fmaf(w0, h2l[bb * 4 + 0][d], fmaf(w1, h2l[bb * 4 + 1][d], fmaf(w2, h2l[bb * 4 + 2][d], w3 * h2l[bb * 4 + 3][d])));
      const float o1 = fmaf(w0, h2l[bb * 4 + 0][d + 1], fmaf(w1, h2l[bb * 4 + 1][d + 1], fmaf(w2, h2l[bb * 4 + 2][d + 1], w3 * h2l[bb * 4 + 3][d + 1])));
      ow[bb * 32 + d] = rbf(o0 / L);
      ow[bb * 32 + d + 1] = rbf(o1 / L);
    }
    asm volatile("s_waitcnt lgkmcnt(0)" ::: "memory");
    __builtin_amdgcn_wave_barrier();
    float y[4][8];
#pragma unroll
    for (int bb = 0; bb < 4; ++bb)
#pragma unroll
      for (int k = 0; k < 8; ++k) y[bb][k] = 0.f;
#pragma unroll
    for (int i = 0; i < 4; ++i)
#pragma unroll
      for (int bb = 0; bb < 4; ++bb) {
        const float ov = ow[bb * 32 + 8 * i + rr];
#pragma unroll
        for (int k = 0; k < 4; ++k) { y[bb][2 * k] = fmaf(ov, bflo(Wr[i][k]), y[bb][2 * k]); y[bb][2 * k + 1] = fmaf(ov, bfhi(Wr[i][k]), y[bb][2 * k + 1]); }
      }
    float z[16];
#pragma unroll
    for (int i = 0; i < 16; ++i) {
      const float lo_ = y[i >> 3][i & 7], hi_ = y[2 + (i >> 3)][i & 7];
      const float give = (lane & 8) ? lo_ : hi_, keep = (lane & 8) ? hi_ : lo_;
      z[i] = keep + dpp<XOR8>(give);
    }
    float z2[8];
#pragma unroll
    for (int i = 0; i < 8; ++i) z2[i] = tr16(z[(i >> 2) * 8 + (i & 3)], z[(i >> 2) * 8 + 4 + (i & 3)]);
    float z3[4];
#pragma unroll
    for (int i = 0; i < 4; ++i) z3[i] = tr32(z2[(i >> 1) * 4 + (i & 1)], z2[(i >> 1) * 4 + 2 + (i & 1)]);
    {
      const int col = 64 * w + 8 * (lane & 7) + 4 * ((lane >> 4) & 1) + 2 * (lane >> 5);
#pragma unroll
      for (int b0 = 0; b0 < 2; ++b0) {
        const int bb = 2 * ((lane >> 3) & 1) + b0;
        const int idx0 = bb * 512 + col;
        const int cns = idx0 >> 4;
        const unsigned tag = elds[cns] + 1u;
        const unsigned off = (unsigned)(WS_H3 + (((size_t)(br * 128 + cns) * 128 + rem) * 16 + (idx0 & 15)) * 8);
        const u32x4 v = {__float_as_uint(z3[2 * b0]), tag, __float_as_uint(z3[2 * b0 + 1]), tag};
        __builtin_amdgcn_raw_buffer_store_b128(v, rsrc, off, 0, 16);
      }
    }
  }
  asm volatile("" ::: "memory");
  STAMP(10);
  {
    const unsigned base = (unsigned)(WS_H3 + (size_t)role * 128 * 16 * 8);
    u32x4 a0, a1;
    for (unsigned spin = 0; spin < SPIN_CAP; ++spin) {
      a0 = __builtin_amdgcn_raw_buffer_load_b128(rsrc, base + (unsigned)tid * 16u, 0, 16);
      a1 = __builtin_amdgcn_raw_buffer_load_b128(rsrc, base + (unsigned)(tid + 512) * 16u, 0, 16);
      const bool ok = (a0[1] == mytag) & (a0[3] == mytag) & (a1[1] == mytag) & (a1[3] == mytag);
      if (__all(ok)) break;
      __builtin_amdgcn_s_sleep(1);
    }
    fin[tid >> 3][2 * (tid & 7)] = __uint_as_float(a0[0]) + __uint_as_float(a1[0]);
    fin[tid >> 3][2 * (tid & 7) + 1] = __uint_as_float(a0[2]) + __uint_as_float(a1[2]);
  }
  STAMP(11);
  wg_barrier();
  if (tid < 16) {
    float t = 0.f;
#pragma unroll
    for (int i = 0; i < 64; ++i) t += fin[i][tid];
    const int idx = rem * 16 + tid, bb = idx >> 9, col = idx & 511;
    p.out[bb * 1024 + br * 512 + col] = f2bf(t);
  }
  if (tid == 0) __hip_atomic_store((gu32*)(ws + WS_E) + role, mytag, RLX_AGENT);
  STAMP(12);
}

extern "C" void kernel_launch(void* const* d_in, const int* in_sizes, int n_in,
                              void* d_out, int out_size, void* d_ws, size_t ws_size,
                              hipStream_t stream) {
  Ptrs p;
  for (int br = 0; br < 2; ++br) {
    p.x[br] = (const unsigned short*)d_in[5 * br + 0];
    p.k[br] = (const unsigned short*)d_in[5 * br + 1];
    p.v[br] = (const unsigned short*)d_in[5 * br + 2];
    p.wq[br] = (const unsigned short*)d_in[5 * br + 3];
    p.wo[br] = (const unsigned short*)d_in[5 * br + 4];
  }
  p.out = (unsigned short*)d_out;
  p.ws = (unsigned char*)d_ws;
  hipLaunchKernelGGL(k_fused, dim3(256), dim3(512), 0, stream, p);
}
```

```cpp
#include <hip/hip_runtime.h>
#include <hip/hip_bf16.h>
#include <stdint.h>

typedef unsigned u32x4 __attribute__((ext_vector_type(4)));
typedef float f32x4 __attribute__((ext_vector_type(4)));
typedef __attribute__((ext_vector_type(2))) __bf16 bf16x2;
typedef unsigned long long u64;
#define GAS __attribute__((address_space(1)))
typedef GAS u64 gu64;
typedef GAS unsigned gu32;
#define RLX_AGENT __ATOMIC_RELAXED, __HIP_MEMORY_SCOPE_AGENT

constexpr int NB = 4, DM = 512, HQ = 32, HKV = 8, SK = 8192, DH = 128, WIN = 2048;
constexpr int NSPLIT = 4, KPS = WIN / NSPLIT;
constexpr float SCALE = 0.08838834764831845f;
constexpr float LOG2E = 1.4426950408889634f;
constexpr unsigned SPIN_CAP = 20000;

constexpr size_t WS_E = 0;
constexpr size_t WS_H1 = 4096;
constexpr size_t WS_H2 = WS_H1 + (size_t)256 * 256 * 8;
constexpr int H2S = 36;
constexpr size_t WS_H3 = WS_H2 + (size_t)256 * 16 * H2S * 8;
constexpr size_t WS_END = WS_H3 + (size_t)256 * 128 * 16 * 8;

__device__ __forceinline__ float bf2f(unsigned short h) { return __uint_as_float(((unsigned)h) << 16); }
__device__ __forceinline__ float bflo(unsigned u) { return __uint_as_float(u << 16); }
__device__ __forceinline__ float bfhi(unsigned u) { return __uint_as_float(u & 0xffff0000u); }
__device__ __forceinline__ float rbf(float f) { __bf16 h = (__bf16)f; return (float)h; }
__device__ __forceinline__ unsigned short f2bf(float f) { __bf16 h = (__bf16)f; return __builtin_bit_cast(unsigned short, h); }
__device__ __forceinline__ float dot2(unsigned k, unsigned q, float acc) {
  return __builtin_amdgcn_fdot2_f32_bf16(__builtin_bit_cast(bf16x2, k), __builtin_bit_cast(bf16x2, q), acc, false);
}
template <int CTRL> __device__ __forceinline__ float dpp(float x) {
  return __builtin_bit_cast(float, __builtin_amdgcn_mov_dpp(__builtin_bit_cast(int, x), CTRL, 0xf, 0xf, true));
}
constexpr int XOR1 = 0xB1, XOR2 = 0x4E, XOR7 = 0x141, XOR8 = 0x128, ROR4 = 0x124;
__device__ __forceinline__ float xrow16_max(float x) {
  auto s = __builtin_amdgcn_permlane16_swap(__float_as_uint(x), __float_as_uint(x), false, false);
  x = fmaxf(__uint_as_float(s[0]), __uint_as_float(s[1]));
  auto t = __builtin_amdgcn_permlane32_swap(__float_as_uint(x), __float_as_uint(x), false, false);
  return fmaxf(__uint_as_float(t[0]), __uint_as_float(t[1]));
}
__device__ __forceinline__ float xrow16_sum(float x) {
  auto s = __builtin_amdgcn_permlane16_swap(__float_as_uint(x), __float_as_uint(x), false, false);
  x = __uint_as_float(s[0]) + __uint_as_float(s[1]);
  auto t = __builtin_amdgcn_permlane32_swap(__float_as_uint(x), __float_as_uint(x), false, false);
  return __uint_as_float(t[0]) + __uint_as_float(t[1]);
}
__device__ __forceinline__ float tr16(float a, float b) {
  auto s = __builtin_amdgcn_permlane16_swap(__float_as_uint(a), __float_as_uint(b), false, false);
  return __uint_as_float(s[0]) + __uint_as_float(s[1]);
}
__device__ __forceinline__ float tr32(float a, float b) {
  auto s = __builtin_amdgcn_permlane32_swap(__float_as_uint(a), __float_as_uint(b), false, false);
  return __uint_as_float(s[0]) + __uint_as_float(s[1]);
}
__device__ __forceinline__ u32x4 ldnt(const void* p) { return __builtin_nontemporal_load(reinterpret_cast<const u32x4*>(p)); }
__device__ __forceinline__ void st_gran(unsigned char* ws, size_t byteoff, unsigned tag, unsigned val) {
  __hip_atomic_store((gu64*)(ws + byteoff), ((u64)tag << 32) | (u64)val, RLX_AGENT);
}
__device__ __forceinline__ u64 ld_gran(const unsigned char* ws, size_t byteoff) {
  return __hip_atomic_load((gu64*)(ws + byteoff), RLX_AGENT);
}
__device__ __forceinline__ void wg_barrier() {
  asm volatile("s_waitcnt lgkmcnt(0)" ::: "memory");
  __builtin_amdgcn_s_barrier();
  asm volatile("" ::: "memory");
}

#define SB() __builtin_amdgcn_sched_barrier(0)
#define STAMP(i) do {} while (0)
struct Ptrs {
  const unsigned short* x[2];
  const unsigned short* k[2];
  const unsigned short* v[2];
  const unsigned short* wq[2];
  const unsigned short* wo[2];
  unsigned short* out;
  unsigned char* ws;
};

__global__ void __launch_bounds__(512, 2) k_fused(Ptrs p) {
  __shared__ __attribute__((aligned(16))) float lds[8 * 4 * 512];
  __shared__ __attribute__((aligned(16))) unsigned qlds[256];
  __shared__ unsigned elds[128];
  __shared__ float wml[8][4][2];
  __shared__ float h2l[16][H2S];
  __shared__ float ol[4][32];
  __shared__ float fin[64][16];
  __shared__ unsigned qcnt;

  const int bid = blockIdx.x;
  const int role = (bid & 7) * 32 + (bid >> 3);
  const int br = role >> 7, rem = role & 127, g = rem >> 4, j = rem & 15, b = j >> 2, s = j & 3;
  const int tid = threadIdx.x, lane = tid & 63;
  const int w = __builtin_amdgcn_readfirstlane(tid >> 6);
  unsigned char* ws = p.ws;
  const int cbase = br * 128 + g * 16;
  const __amdgpu_buffer_rsrc_t rsrc = __builtin_amdgcn_make_buffer_rsrc(ws, 0, (int)WS_END, 0x00020000);

  STAMP(0);
  if (tid == 0) qcnt = 0u;
  const unsigned eword = __hip_atomic_load((gu32*)(ws + WS_E) + br * 128 + (tid & 127), RLX_AGENT);
  const int r = lane >> 4, c = lane & 15;
  const size_t kvbase = ((size_t)(b * HKV + g) * SK + (SK - WIN) + s * KPS + w * 64) * DH;
  const unsigned short* kp = p.k[br] + kvbase + (size_t)r * DH + 8 * c;
  const unsigned short* vp = p.v[br] + kvbase + (size_t)r * DH + 8 * c;
  u32x4 Kr[16], Vr[16];
  wg_barrier();
  unsigned mytag = 0;
  if (w < 4) {
    __builtin_amdgcn_s_setprio(3);
    const int qr = lane >> 2, qc = lane & 3;
    u32x4 W[8];
    {
      const unsigned short* wq = p.wq[br] + (size_t)(128 * w + qr) * 4096 + g * 512 + j * 32 + 8 * qc;
#pragma unroll
      for (int t = 0; t < 8; ++t) W[t] = ldnt(wq + (size_t)(16 * t) * 4096);
    }
    unsigned short xr[8][4];
    {
      const unsigned short* x = p.x[br] + 128 * w + qr;
#pragma unroll
      for (int t = 0; t < 8; ++t)
#pragma unroll
        for (int bb = 0; bb < 4; ++bb) xr[t][bb] = x[bb * DM + 16 * t];
    }
    if (tid < 128) elds[tid] = eword;
    float acc[4][8];
#pragma unroll
    for (int bb = 0; bb < 4; ++bb)
#pragma unroll
      for (int k = 0; k < 8; ++k) acc[bb][k] = 0.f;
#pragma unroll
    for (int t = 0; t < 8; ++t)
#pragma unroll
      for (int bb = 0; bb < 4; ++bb) {
        const float xv = bf2f(xr[t][bb]);
#pragma unroll
        for (int k = 0; k < 4; ++k) {
          acc[bb][2 * k] = fmaf(xv, bflo(W[t][k]), acc[bb][2 * k]);
          acc[bb][2 * k + 1] = fmaf(xv, bfhi(W[t][k]), acc[bb][2 * k + 1]);
        }
      }
#pragma unroll
    for (int bb = 0; bb < 4; ++bb)
#pragma unroll
      for (int k = 0; k < 8; ++k) {
        float v = acc[bb][k];
        v += dpp<ROR4>(v); v += dpp<XOR8>(v); v = xrow16_sum(v);
        acc[bb][k] = v;
      }
    if (qr == 0) {
      float* red = lds + w * 128;
#pragma unroll
      for (int bb = 0; bb < 4; ++bb)
#pragma unroll
        for (int k = 0; k < 8; ++k) red[bb * 32 + 8 * qc + k] = acc[bb][k];
    }
    asm volatile("s_waitcnt lgkmcnt(0)" ::: "memory");
    if (lane == 0) __hip_atomic_fetch_add(&qcnt, 1u, __ATOMIC_RELAXED, __HIP_MEMORY_SCOPE_WORKGROUP);
    STAMP(1);
  }
  const size_t h1base = WS_H1 + (size_t)role * 256 * 8;
  u64 g0 = 0, g1 = 0, g2 = 0, g3 = 0;
  if (w == 0) {
    for (unsigned spin = 0; spin < 1000000u; ++spin) {
      const unsigned cnt = __hip_atomic_load(&qcnt, __ATOMIC_RELAXED, __HIP_MEMORY_SCOPE_WORKGROUP);
      if (cnt >= 4u) break;
    }
    asm volatile("" ::: "memory");
#pragma unroll
    for (int k = 0; k < 4; ++k) {
      const int idx = k * 64 + lane, cc = idx >> 4, i = idx & 15, bb = cc >> 2;
      float s0 = 0.f, s1 = 0.f;
#pragma unroll
      for (int ww = 0; ww < 4; ++ww) { s0 += lds[ww * 128 + bb * 32 + 2 * i]; s1 += lds[ww * 128 + bb * 32 + 2 * i + 1]; }
      const unsigned val = (unsigned)f2bf(s0) | ((unsigned)f2bf(s1) << 16);
      st_gran(ws, WS_H1 + ((size_t)(cbase + cc) * 256 + j * 16 + i) * 8, elds[g * 16 + cc] + 1u, val);
    }
    STAMP(2);
    g0 = ld_gran(ws, h1base + (size_t)(0 * 64 + lane) * 8);
    g1 = ld_gran(ws, h1base + (size_t)(1 * 64 + lane) * 8);
    g2 = ld_gran(ws, h1base + (size_t)(2 * 64 + lane) * 8);
    g3 = ld_gran(ws, h1base + (size_t)(3 * 64 + lane) * 8);
  }
  SB();
  if (w != 0) __builtin_amdgcn_s_setprio(0);
  unsigned touch0, touch1;
  {
    size_t o0, o1;
    if (w == 4) { o0 = WS_H3 + ((size_t)(br * 128 + lane) * 128 + rem) * 128; o1 = WS_H3 + ((size_t)(br * 128 + 64 + lane) * 128 + rem) * 128; }
    else if (w == 5) { o0 = WS_H3 + (size_t)role * 16384 + (size_t)lane * 256; o1 = o0 + 128; }
    else if (w == 6) { o0 = WS_H2 + ((size_t)(cbase + (lane & 15)) * 16 + j) * H2S * 8 + (size_t)(lane >> 4) * 64; o1 = WS_H2 + (size_t)role * 16 * H2S * 8 + (size_t)lane * 64; }
    else { o0 = WS_H1 + (size_t)role * 2048 + (size_t)(lane & 15) * 128; o1 = o0; }
    touch0 = __hip_atomic_load((gu32*)(ws + o0), RLX_AGENT);
    touch1 = __hip_atomic_load((gu32*)(ws + o1), RLX_AGENT);
  }
  SB();
#pragma unroll
  for (int t = 0; t < 16; ++t) { Kr[t] = ldnt(kp + (size_t)t * 4 * DH); SB(); }
  if (w == 0) {
    mytag = elds[g * 16 + j] + 1u;
    const bool ok0 = ((unsigned)(g0 >> 32) == mytag) & ((unsigned)(g1 >> 32) == mytag) & ((unsigned)(g2 >> 32) == mytag) & ((unsigned)(g3 >> 32) == mytag);
    if (!__all(ok0)) {
      for (unsigned spin = 0; spin < SPIN_CAP; ++spin) {
        g0 = ld_gran(ws, h1base + (size_t)(0 * 64 + lane) * 8);
        g1 = ld_gran(ws, h1base + (size_t)(1 * 64 + lane) * 8);
        g2 = ld_gran(ws, h1base + (size_t)(2 * 64 + lane) * 8);
        g3 = ld_gran(ws, h1base + (size_t)(3 * 64 + lane) * 8);
        const bool ok = ((unsigned)(g0 >> 32) == mytag) & ((unsigned)(g1 >> 32) == mytag) & ((unsigned)(g2 >> 32) == mytag) & ((unsigned)(g3 >> 32) == mytag);
        if (__all(ok)) break;
        __builtin_amdgcn_s_sleep(1);
      }
    }
    qlds[0 * 64 + lane] = (unsigned)g0; qlds[1 * 64 + lane] = (unsigned)g1; qlds[2 * 64 + lane] = (unsigned)g2; qlds[3 * 64 + lane] = (unsigned)g3;
  }
  __builtin_amdgcn_s_setprio(0);
  STAMP(3);
  wg_barrier();
  STAMP(4);
  asm volatile("" :: "v"(touch0), "v"(touch1));
  mytag = elds[g * 16 + j] + 1u;
  u32x4 qp[4];
#pragma unroll
  for (int h = 0; h < 4; ++h) qp[h] = *reinterpret_cast<const u32x4*>(&qlds[h * 64 + 4 * c]);
  float sc[4];
#pragma unroll
  for (int gi = 0; gi < 4; ++gi) {
    float x[16];
#pragma unroll
    for (int tl = 0; tl < 4; ++tl)
#pragma unroll
      for (int h = 0; h < 4; ++h) {
        float d = 0.f;
#pragma unroll
        for (int k = 0; k < 4; ++k) d = dot2(Kr[4 * gi + tl][k], qp[h][k], d);
        x[tl * 4 + h] = d;
      }
    SB();
#pragma unroll
    for (int tl = 0; tl < 4; ++tl) { Vr[4 * gi + tl] = ldnt(vp + (size_t)(4 * gi + tl) * 4 * DH); SB(); }
#define TR_STEP(HALF, CTRL, BIT) _Pragma("unroll") for (int hh = 0; hh < (HALF); ++hh) { \
      const float lo_ = x[hh], hi_ = x[hh + (HALF)]; const float t1_ = lo_ + dpp<CTRL>(lo_), t2_ = hi_ + dpp<CTRL>(hi_); \
      x[hh] = (c & (BIT)) ? t2_ : t1_; }
    TR_STEP(8, XOR8, 8) TR_STEP(4, XOR7, 4) TR_STEP(2, XOR2, 2) TR_STEP(1, XOR1, 1)
#undef TR_STEP
    sc[gi] = rbf(rbf(x[0]) * SCALE);
  }
  u32x4 Wr[4];
  {
    const unsigned short* wo = p.wo[br] + (size_t)(g * 512 + j * 32 + (lane >> 3)) * DM + 64 * w + 8 * (lane & 7);
    SB();
#pragma unroll
    for (int i = 0; i < 4; ++i) Wr[i] = ldnt(wo + (size_t)(8 * i) * DM);
    SB();
  }
  STAMP(5);
  float m = fmaxf(fmaxf(sc[0], sc[1]), fmaxf(sc[2], sc[3]));
  m = fmaxf(m, dpp<ROR4>(m)); m = fmaxf(m, dpp<XOR8>(m)); m = xrow16_max(m);
  unsigned short* pl = reinterpret_cast<unsigned short*>(lds + w * 256);
  float l = 0.f;
#pragma unroll
  for (int gi = 0; gi < 4; ++gi) {
    const float pv = __builtin_amdgcn_exp2f((sc[gi] - m) * LOG2E);
    l += pv;
    const int t = 4 * gi + (c >> 2);
    pl[(((t >> 1) * 4 + r) * 4 + (c & 3)) * 2 + (t & 1)] = f2bf(pv);
  }
  l += dpp<ROR4>(l); l += dpp<XOR8>(l); l = xrow16_sum(l);
  if (lane < 4) { wml[w][lane][0] = m; wml[w][lane][1] = l; }
  asm volatile("s_waitcnt lgkmcnt(0)" ::: "memory");
  __builtin_amdgcn_wave_barrier();
  float acc[32];
#pragma unroll
  for (int k = 0; k < 32; ++k) acc[k] = 0.f;
#pragma unroll
  for (int u = 0; u < 8; ++u) {
    const u32x4 pr = *reinterpret_cast<const u32x4*>(&pl[((u * 4 + r) * 4) * 2]);
    unsigned vv[8];
#pragma unroll
    for (int k = 0; k < 4; ++k) {
      vv[2 * k] = __builtin_amdgcn_perm(Vr[2 * u + 1][k], Vr[2 * u][k], 0x05040100u);
      vv[2 * k + 1] = __builtin_amdgcn_perm(Vr[2 * u + 1][k], Vr[2 * u][k], 0x07060302u);
    }
#pragma unroll
    for (int h = 0; h < 4; ++h)
#pragma unroll
      for (int k = 0; k < 8; ++k) acc[h * 8 + k] = dot2(vv[k], pr[h], acc[h * 8 + k]);
  }
#pragma unroll
  for (int i = 0; i < 16; ++i) acc[i] = tr16(acc[i], acc[i + 16]);
#pragma unroll
  for (int i = 0; i < 8; ++i) acc[i] = tr32(acc[i], acc[i + 8]);
  STAMP(6);
  float* accl = lds + 2048;
  {
    const int hh = 2 * (r & 1) + (r >> 1);
    *reinterpret_cast<f32x4*>(&accl[(w * 4 + hh) * 128 + 8 * c]) = f32x4{acc[0], acc[1], acc[2], acc[3]};
    *reinterpret_cast<f32x4*>(&accl[(w * 4 + hh) * 128 + 8 * c + 4]) = f32x4{acc[4], acc[5], acc[6], acc[7]};
  }
  wg_barrier();
  STAMP(7);
  {
    const int jc = tid >> 5, d = tid & 31, h = jc >> 2, dq = jc & 3;
    float M = wml[0][h][0];
#pragma unroll
    for (int ww = 1; ww < 8; ++ww) M = fmaxf(M, wml[ww][h][0]);
    float o = 0.f, L = 0.f;
#pragma unroll
    for (int ww = 0; ww < 8; ++ww) {
      const float wt = __builtin_amdgcn_exp2f((wml[ww][h][0] - M) * LOG2E);
      o = fmaf(wt, accl[(ww * 4 + h) * 128 + dq * 32 + d], o);
      L = fmaf(wt, wml[ww][h][1], L);
    }
    const unsigned tag = elds[g * 16 + jc] + 1u;
    const size_t base = WS_H2 + ((size_t)(cbase + jc) * 16 + j) * H2S * 8;
    st_gran(ws, base + (size_t)d * 8, tag, __float_as_uint(o));
    if (d < 2) st_gran(ws, base + (size_t)(32 + d) * 8, tag, __float_as_uint(d == 0 ? M : L));
  }
  asm volatile("" ::: "memory");
  STAMP(8);
  {
    const size_t base = WS_H2 + (size_t)role * 16 * H2S * 8;
    const int pp = 2 * w + (lane >= 34 ? 1 : 0);
    const int e0 = lane >= 34 ? lane - 34 : lane;
    unsigned v0 = 0, v1 = 0;
    for (unsigned spin = 0; spin < SPIN_CAP; ++spin) {
      bool ok = true;
      const u64 x0 = ld_gran(ws, base + (size_t)(pp * H2S + e0) * 8); v0 = (unsigned)x0; ok &= ((unsigned)(x0 >> 32) == mytag);
      if (lane < 4) { const u64 x1 = ld_gran(ws, base + (size_t)((2 * w + 1) * H2S + 30 + lane) * 8); v1 = (unsigned)x1; ok &= ((unsigned)(x1 >> 32) == mytag); }
      if (__all(ok)) break;
      __builtin_amdgcn_s_sleep(1);
    }
    h2l[pp][e0] = __uint_as_float(v0);
    if (lane < 4) h2l[2 * w + 1][30 + lane] = __uint_as_float(v1);
  }
  STAMP(9);
  wg_barrier();
  {
    const int rr = lane >> 3;
    float* ow = lds + 2048 + w * 128;
    {
      const int bb = lane >> 4, d = 2 * (lane & 15);
      const float m0 = h2l[bb * 4 + 0][32], m1 = h2l[bb * 4 + 1][32], m2 = h2l[bb * 4 + 2][32], m3 = h2l[bb * 4 + 3][32];
      const float M = fmaxf(fmaxf(m0, m1), fmaxf(m2, m3));
      const float w0 = __builtin_amdgcn_exp2f((m0 - M) * LOG2E), w1 = __builtin_amdgcn_exp2f((m1 - M) * LOG2E);
      const float w2 = __builtin_amdgcn_exp2f((m2 - M) * LOG2E), w3 = __builtin_amdgcn_exp2f((m3 - M) * LOG2E);
      const float L = fmaf(w0, h2l[bb * 4 + 0][33], fmaf(w1, h2l[bb * 4 + 1][33], fmaf(w2, h2l[bb * 4 + 2][33], w3 * h2l[bb * 4 + 3][33])));
      const float o0 = fmaf(w0, h2l[bb * 4 + 0][d], fmaf(w1, h2l[bb * 4 + 1][d], fmaf(w2, h2l[bb * 4 + 2][d], w3 * h2l[bb * 4 + 3][d])));
      const float o1 = fmaf(w0, h2l[bb * 4 + 0][d + 1], fmaf(w1, h2l[bb * 4 + 1][d + 1], fmaf(w2, h2l[bb * 4 + 2][d + 1], w3 * h2l[bb * 4 + 3][d + 1])));
      ow[bb * 32 + d] = rbf(o0 / L);
      ow[bb * 32 + d + 1] = rbf(o1 / L);
    }
    asm volatile("s_waitcnt lgkmcnt(0)" ::: "memory");
    __builtin_amdgcn_wave_barrier();
    float y[4][8];
#pragma unroll
    for (int bb = 0; bb < 4; ++bb)
#pragma unroll
      for (int k = 0; k < 8; ++k) y[bb][k] = 0.f;
#pragma unroll
    for (int i = 0; i < 4; ++i)
#pragma unroll
      for (int bb = 0; bb < 4; ++bb) {
        const float ov = ow[bb * 32 + 8 * i + rr];
#pragma unroll
        for (int k = 0; k < 4; ++k) { y[bb][2 * k] = fmaf(ov, bflo(Wr[i][k]), y[bb][2 * k]); y[bb][2 * k + 1] = fmaf(ov, bfhi(Wr[i][k]), y[bb][2 * k + 1]); }
      }
    float z[16];
#pragma unroll
    for (int i = 0; i < 16; ++i) {
      const float lo_ = y[i >> 3][i & 7], hi_ = y[2 + (i >> 3)][i & 7];
      const float give = (lane & 8) ? lo_ : hi_, keep = (lane & 8) ? hi_ : lo_;
      z[i] = keep + dpp<XOR8>(give);
    }
    float z2[8];
#pragma unroll
    for (int i = 0; i < 8; ++i) z2[i] = tr16(z[(i >> 2) * 8 + (i & 3)], z[(i >> 2) * 8 + 4 + (i & 3)]);
    float z3[4];
#pragma unroll
    for (int i = 0; i < 4; ++i) z3[i] = tr32(z2[(i >> 1) * 4 + (i & 1)], z2[(i >> 1) * 4 + 2 + (i & 1)]);
    {
      const int col = 64 * w + 8 * (lane & 7) + 4 * ((lane >> 4) & 1) + 2 * (lane >> 5);
#pragma unroll
      for (int b0 = 0; b0 < 2; ++b0) {
        const int bb = 2 * ((lane >> 3) & 1) + b0;
        const int idx0 = bb * 512 + col;
        const int cns = idx0 >> 4;
        const unsigned tag = elds[cns] + 1u;
        const unsigned off = (unsigned)(WS_H3 + (((size_t)(br * 128 + cns) * 128 + rem) * 16 + (idx0 & 15)) * 8);
        const u32x4 v = {__float_as_uint(z3[2 * b0]), tag, __float_as_uint(z3[2 * b0 + 1]), tag};
        __builtin_amdgcn_raw_buffer_store_b128(v, rsrc, off, 0, 16);
      }
    }
  }
  asm volatile("" ::: "memory");
  STAMP(10);
  {
    const unsigned base = (unsigned)(WS_H3 + (size_t)role * 128 * 16 * 8);
    u32x4 a0, a1;
    for (unsigned spin = 0; spin < SPIN_CAP; ++spin) {
      a0 = __builtin_amdgcn_raw_buffer_load_b128(rsrc, base + (unsigned)tid * 16u, 0, 16);
      a1 = __builtin_amdgcn_raw_buffer_load_b128(rsrc, base + (unsigned)(tid + 512) * 16u, 0, 16);
      const bool ok = (a0[1] == mytag) & (a0[3] == mytag) & (a1[1] == mytag) & (a1[3] == mytag);
      if (__all(ok)) break;
      __builtin_amdgcn_s_sleep(1);
    }
    fin[tid >> 3][2 * (tid & 7)] = __uint_as_float(a0[0]) + __uint_as_float(a1[0]);
    fin[tid >> 3][2 * (tid & 7) + 1] = __uint_as_float(a0[2]) + __uint_as_float(a1[2]);
  }
  STAMP(11);
  wg_barrier();
  if (tid < 16) {
    float t = 0.f;
#pragma unroll
    for (int i = 0; i < 64; ++i) t += fin[i][tid];
    const int idx = rem * 16 + tid, bb = idx >> 9, col = idx & 511;
    p.out[bb * 1024 + br * 512 + col] = f2bf(t);
  }
  if (tid == 0) __hip_atomic_store((gu32*)(ws + WS_E) + role, mytag, RLX_AGENT);
  STAMP(12);
}

extern "C" void kernel_launch(void* const* d_in, const int* in_sizes, int n_in,
                              void* d_out, int out_size, void* d_ws, size_t ws_size,
                              hipStream_t stream) {
  Ptrs p;
  for (int br = 0; br < 2; ++br) {
    p.x[br] = (const unsigned short*)d_in[5 * br + 0];
    p.k[br] = (const unsigned short*)d_in[5 * br + 1];
    p.v[br] = (const unsigned short*)d_in[5 * br + 2];
    p.wq[br] = (const unsigned short*)d_in[5 * br + 3];
    p.wo[br] = (const unsigned short*)d_in[5 * br + 4];
  }
  p.out = (unsigned short*)d_out;
  p.ws = (unsigned char*)d_ws;
  hipLaunchKernelGGL(k_fused, dim3(256), dim3(512), 0, stream, p);
}
```

```cpp
#include <hip/hip_runtime.h>
#include <hip/hip_bf16.h>
#include <stdint.h>

typedef unsigned u32x4 __attribute__((ext_vector_type(4)));
typedef float f32x4 __attribute__((ext_vector_type(4)));
typedef __attribute__((ext_vector_type(2))) __bf16 bf16x2;
typedef unsigned long long u64;
#define GAS __attribute__((address_space(1)))
typedef GAS u64 gu64;
typedef GAS unsigned gu32;
#define RLX_AGENT __ATOMIC_RELAXED, __HIP_MEMORY_SCOPE_AGENT

constexpr int NB = 4, DM = 512, HQ = 32, HKV = 8, SK = 8192, DH = 128, WIN = 2048;
constexpr int NSPLIT = 4, KPS = WIN / NSPLIT;
constexpr float SCALE = 0.08838834764831845f;
constexpr float LOG2E = 1.4426950408889634f;
constexpr unsigned SPIN_CAP = 20000;

constexpr size_t WS_E = 0;
constexpr size_t WS_H1 = 4096;
constexpr size_t WS_H2 = WS_H1 + (size_t)256 * 256 * 8;
constexpr int H2S = 36;
constexpr size_t WS_H3 = WS_H2 + (size_t)256 * 16 * H2S * 8;
constexpr size_t WS_END = WS_H3 + (size_t)256 * 128 * 16 * 8;

__device__ __forceinline__ float bf2f(unsigned short h) { return __uint_as_float(((unsigned)h) << 16); }
__device__ __forceinline__ float bflo(unsigned u) { return __uint_as_float(u << 16); }
__device__ __forceinline__ float bfhi(unsigned u) { return __uint_as_float(u & 0xffff0000u); }
__device__ __forceinline__ float rbf(float f) { __bf16 h = (__bf16)f; return (float)h; }
__device__ __forceinline__ unsigned short f2bf(float f) { __bf16 h = (__bf16)f; return __builtin_bit_cast(unsigned short, h); }
__device__ __forceinline__ float dot2(unsigned k, unsigned q, float acc) {
  return __builtin_amdgcn_fdot2_f32_bf16(__builtin_bit_cast(bf16x2, k), __builtin_bit_cast(bf16x2, q), acc, false);
}
template <int CTRL> __device__ __forceinline__ float dpp(float x) {
  return __builtin_bit_cast(float, __builtin_amdgcn_mov_dpp(__builtin_bit_cast(int, x), CTRL, 0xf, 0xf, true));
}
constexpr int XOR1 = 0xB1, XOR2 = 0x4E, XOR7 = 0x141, XOR8 = 0x128, ROR4 = 0x124;
__device__ __forceinline__ float xrow16_max(float x) {
  auto s = __builtin_amdgcn_permlane16_swap(__float_as_uint(x), __float_as_uint(x), false, false);
  x = fmaxf(__uint_as_float(s[0]), __uint_as_float(s[1]));
  auto t = __builtin_amdgcn_permlane32_swap(__float_as_uint(x), __float_as_uint(x), false, false);
  return fmaxf(__uint_as_float(t[0]), __uint_as_float(t[1]));
}
__device__ __forceinline__ float xrow16_sum(float x) {
  auto s = __builtin_amdgcn_permlane16_swap(__float_as_uint(x), __float_as_uint(x), false, false);
  x = __uint_as_float(s[0]) + __uint_as_float(s[1]);
  auto t = __builtin_amdgcn_permlane32_swap(__float_as_uint(x), __float_as_uint(x), false, false);
  return __uint_as_float(t[0]) + __uint_as_float(t[1]);
}
__device__ __forceinline__ float tr16(float a, float b) {
  auto s = __builtin_amdgcn_permlane16_swap(__float_as_uint(a), __float_as_uint(b), false, false);
  return __uint_as_float(s[0]) + __uint_as_float(s[1]);
}
__device__ __forceinline__ float tr32(float a, float b) {
  auto s = __builtin_amdgcn_permlane32_swap(__float_as_uint(a), __float_as_uint(b), false, false);
  return __uint_as_float(s[0]) + __uint_as_float(s[1]);
}
__device__ __forceinline__ u32x4 ldnt(const void* p) { return __builtin_nontemporal_load(reinterpret_cast<const u32x4*>(p)); }
__device__ __forceinline__ void st_gran(unsigned char* ws, size_t byteoff, unsigned tag, unsigned val) {
  __hip_atomic_store((gu64*)(ws + byteoff), ((u64)tag << 32) | (u64)val, RLX_AGENT);
}
__device__ __forceinline__ u64 ld_gran(const unsigned char* ws, size_t byteoff) {
  return __hip_atomic_load((gu64*)(ws + byteoff), RLX_AGENT);
}
__device__ __forceinline__ void wg_barrier() {
  asm volatile("s_waitcnt lgkmcnt(0)" ::: "memory");
  __builtin_amdgcn_s_barrier();
  asm volatile("" ::: "memory");
}

#define SB() __builtin_amdgcn_sched_barrier(0)
#define STAMP(i) do {} while (0)
struct Ptrs {
  const unsigned short* x[2];
  const unsigned short* k[2];
  const unsigned short* v[2];
  const unsigned short* wq[2];
  const unsigned short* wo[2];
  unsigned short* out;
  unsigned char* ws;
};

__global__ void __launch_bounds__(512, 2) k_fused(Ptrs p) {
  __shared__ __attribute__((aligned(16))) float lds[8 * 4 * 512];
  __shared__ __attribute__((aligned(16))) unsigned qlds[256];
  __shared__ unsigned elds[128];
  __shared__ float wml[8][4][2];
  __shared__ float h2l[16][H2S];
  __shared__ float ol[4][32];
  __shared__ float fin[64][16];
  __shared__ unsigned qcnt;

  const int bid = blockIdx.x;
  const int role = (bid & 7) * 32 + (bid >> 3);
  const int br = role >> 7, rem = role & 127, g = rem >> 4, j = rem & 15, b = j >> 2, s = j & 3;
  const int tid = threadIdx.x, lane = tid & 63;
  const int w = __builtin_amdgcn_readfirstlane(tid >> 6);
  unsigned char* ws = p.ws;
  const int cbase = br * 128 + g * 16;
  const __amdgpu_buffer_rsrc_t rsrc = __builtin_amdgcn_make_buffer_rsrc(ws, 0, (int)WS_END, 0x00020000);

  STAMP(0);
  if (tid == 0) qcnt = 0u;
  const unsigned eword = __hip_atomic_load((gu32*)(ws + WS_E) + br * 128 + (tid & 127), RLX_AGENT);
  const int r = lane >> 4, c = lane & 15;
  const size_t kvbase = ((size_t)(b * HKV + g) * SK + (SK - WIN) + s * KPS + w * 64) * DH;
  const unsigned short* kp = p.k[br] + kvbase + (size_t)r * DH + 8 * c;
  const unsigned short* vp = p.v[br] + kvbase + (size_t)r * DH + 8 * c;
  u32x4 Kr[16], Vr[16];
  wg_barrier();
  unsigned mytag = 0;
  if (w < 4) {
    __builtin_amdgcn_s_setprio(3);
    const int qr = lane >> 2, qc = lane & 3;
    u32x4 W[8];
    {
      const unsigned short* wq = p.wq[br] + (size_t)(128 * w + qr) * 4096 + g * 512 + j * 32 + 8 * qc;
#pragma unroll
      for (int t = 0; t < 8; ++t) W[t] = ldnt(wq + (size_t)(16 * t) * 4096);
    }
    unsigned short xr[8][4];
    {
      const unsigned short* x = p.x[br] + 128 * w + qr;
#pragma unroll
      for (int t = 0; t < 8; ++t)
#pragma unroll
        for (int bb = 0; bb < 4; ++bb) xr[t][bb] = x[bb * DM + 16 * t];
    }
    if (tid < 128) elds[tid] = eword;
    float acc[4][8];
#pragma unroll
    for (int bb = 0; bb < 4; ++bb)
#pragma unroll
      for (int k = 0; k < 8; ++k) acc[bb][k] = 0.f;
#pragma unroll
    for (int t = 0; t < 8; ++t)
#pragma unroll
      for (int bb = 0; bb < 4; ++bb) {
        const float xv = bf2f(xr[t][bb]);
#pragma unroll
        for (int k = 0; k < 4; ++k) {
          acc[bb][2 * k] = fmaf(xv, bflo(W[t][k]), acc[bb][2 * k]);
          acc[bb][2 * k + 1] = fmaf(xv, bfhi(W[t][k]), acc[bb][2 * k + 1]);
        }
      }
#pragma unroll
    for (int bb = 0; bb < 4; ++bb)
#pragma unroll
      for (int k = 0; k < 8; ++k) {
        float v = acc[bb][k];
        v += dpp<ROR4>(v); v += dpp<XOR8>(v); v = xrow16_sum(v);
        acc[bb][k] = v;
      }
    if (qr == 0) {
      float* red = lds + w * 128;
#pragma unroll
      for (int bb = 0; bb < 4; ++bb)
#pragma unroll
        for (int k = 0; k < 8; ++k) red[bb * 32 + 8 * qc + k] = acc[bb][k];
    }
    asm volatile("s_waitcnt lgkmcnt(0)" ::: "memory");
    if (lane == 0) __hip_atomic_fetch_add(&qcnt, 1u, __ATOMIC_RELAXED, __HIP_MEMORY_SCOPE_WORKGROUP);
    STAMP(1);
  }
  const size_t h1base = WS_H1 + (size_t)role * 256 * 8;
  u64 g0 = 0, g1 = 0, g2 = 0, g3 = 0;
  if (w == 0) {
    for (unsigned spin = 0; spin < 1000000u; ++spin) {
      const unsigned cnt = __hip_atomic_load(&qcnt, __ATOMIC_RELAXED, __HIP_MEMORY_SCOPE_WORKGROUP);
      if (cnt >= 4u) break;
    }
    asm volatile("" ::: "memory");
#pragma unroll
    for (int k = 0; k < 4; ++k) {
      const int idx = k * 64 + lane, cc = idx >> 4, i = idx & 15, bb = cc >> 2;
      float s0 = 0.f, s1 = 0.f;
#pragma unroll
      for (int ww = 0; ww < 4; ++ww) { s0 += lds[ww * 128 + bb * 32 + 2 * i]; s1 += lds[ww * 128 + bb * 32 + 2 * i + 1]; }
      const unsigned val = (unsigned)f2bf(s0) | ((unsigned)f2bf(s1) << 16);
      st_gran(ws, WS_H1 + ((size_t)(cbase + cc) * 256 + j * 16 + i) * 8, elds[g * 16 + cc] + 1u, val);
    }
    STAMP(2);
    g0 = ld_gran(ws, h1base + (size_t)(0 * 64 + lane) * 8);
    g1 = ld_gran(ws, h1base + (size_t)(1 * 64 + lane) * 8);
    g2 = ld_gran(ws, h1base + (size_t)(2 * 64 + lane) * 8);
    g3 = ld_gran(ws, h1base + (size_t)(3 * 64 + lane) * 8);
  }
  SB();
  if (w != 0) __builtin_amdgcn_s_setprio(0);
  unsigned touch0, touch1, touch2, touch3;
  {
    size_t o0, o1;
    if (w == 4) { o0 = WS_H3 + ((size_t)(br * 128 + lane) * 128 + rem) * 128; o1 = WS_H3 + ((size_t)(br * 128 + 64 + lane) * 128 + rem) * 128; }
    else if (w == 5) { o0 = WS_H3 + (size_t)role * 16384 + (size_t)lane * 256; o1 = o0 + 128; }
    else if (w == 6) { o0 = WS_H2 + ((size_t)(cbase + (lane & 15)) * 16 + j) * H2S * 8 + (size_t)(lane >> 4) * 64; o1 = WS_H2 + (size_t)role * 16 * H2S * 8 + (size_t)lane * 64; }
    else { o0 = WS_H1 + (size_t)role * 2048 + (size_t)(lane & 15) * 128; o1 = o0; }
    touch0 = __hip_atomic_load((gu32*)(ws + o0), RLX_AGENT);
    touch1 = __hip_atomic_load((gu32*)(ws + o1), RLX_AGENT);
    touch2 = *reinterpret_cast<const unsigned*>(p.v[br] + kvbase + (size_t)(lane & 3) * 2048);
    touch3 = *reinterpret_cast<const unsigned*>(p.wo[br] + (size_t)(g * 512 + j * 32 + 4 * (lane & 7)) * DM);
  }
  SB();
#pragma unroll
  for (int t = 0; t < 16; ++t) { Kr[t] = ldnt(kp + (size_t)t * 4 * DH); SB(); }
  if (w == 0) {
    mytag = elds[g * 16 + j] + 1u;
    const bool ok0 = ((unsigned)(g0 >> 32) == mytag) & ((unsigned)(g1 >> 32) == mytag) & ((unsigned)(g2 >> 32) == mytag) & ((unsigned)(g3 >> 32) == mytag);
    if (!__all(ok0)) {
      for (unsigned spin = 0; spin < SPIN_CAP; ++spin) {
        g0 = ld_gran(ws, h1base + (size_t)(0 * 64 + lane) * 8);
        g1 = ld_gran(ws, h1base + (size_t)(1 * 64 + lane) * 8);
        g2 = ld_gran(ws, h1base + (size_t)(2 * 64 + lane) * 8);
        g3 = ld_gran(ws, h1base + (size_t)(3 * 64 + lane) * 8);
        const bool ok = ((unsigned)(g0 >> 32) == mytag) & ((unsigned)(g1 >> 32) == mytag) & ((unsigned)(g2 >> 32) == mytag) & ((unsigned)(g3 >> 32) == mytag);
        if (__all(ok)) break;
        __builtin_amdgcn_s_sleep(1);
      }
    }
    qlds[0 * 64 + lane] = (unsigned)g0; qlds[1 * 64 + lane] = (unsigned)g1; qlds[2 * 64 + lane] = (unsigned)g2; qlds[3 * 64 + lane] = (unsigned)g3;
  }
  __builtin_amdgcn_s_setprio(0);
  STAMP(3);
  wg_barrier();
  STAMP(4);
  asm volatile("" :: "v"(touch0), "v"(touch1), "v"(touch2), "v"(touch3));
  mytag = elds[g * 16 + j] + 1u;
  u32x4 qp[4];
#pragma unroll
  for (int h = 0; h < 4; ++h) qp[h] = *reinterpret_cast<const u32x4*>(&qlds[h * 64 + 4 * c]);
  float sc[4];
#pragma unroll
  for (int gi = 0; gi < 4; ++gi) {
    float x[16];
#pragma unroll
    for (int tl = 0; tl < 4; ++tl)
#pragma unroll
      for (int h = 0; h < 4; ++h) {
        float d = 0.f;
#pragma unroll
        for (int k = 0; k < 4; ++k) d = dot2(Kr[4 * gi + tl][k], qp[h][k], d);
        x[tl * 4 + h] = d;
      }
    SB();
#pragma unroll
    for (int tl = 0; tl < 4; ++tl) { Vr[4 * gi + tl] = ldnt(vp + (size_t)(4 * gi + tl) * 4 * DH); SB(); }
#define TR_STEP(HALF, CTRL, BIT) _Pragma("unroll") for (int hh = 0; hh < (HALF); ++hh) { \
      const float lo_ = x[hh], hi_ = x[hh + (HALF)]; const float t1_ = lo_ + dpp<CTRL>(lo_), t2_ = hi_ + dpp<CTRL>(hi_); \
      x[hh] = (c & (BIT)) ? t2_ : t1_; }
    TR_STEP(8, XOR8, 8) TR_STEP(4, XOR7, 4) TR_STEP(2, XOR2, 2) TR_STEP(1, XOR1, 1)
#undef TR_STEP
    sc[gi] = rbf(rbf(x[0]) * SCALE);
  }
  u32x4 Wr[4];
  {
    const unsigned short* wo = p.wo[br] + (size_t)(g * 512 + j * 32 + (lane >> 3)) * DM + 64 * w + 8 * (lane & 7);
    SB();
#pragma unroll
    for (int i = 0; i < 4; ++i) Wr[i] = ldnt(wo + (size_t)(8 * i) * DM);
    SB();
  }
  STAMP(5);
  float m = fmaxf(fmaxf(sc[0], sc[1]), fmaxf(sc[2], sc[3]));
  m = fmaxf(m, dpp<ROR4>(m)); m = fmaxf(m, dpp<XOR8>(m)); m = xrow16_max(m);
  unsigned short* pl = reinterpret_cast<unsigned short*>(lds + w * 256);
  float l = 0.f;
#pragma unroll
  for (int gi = 0; gi < 4; ++gi) {
    const float pv = __builtin_amdgcn_exp2f((sc[gi] - m) * LOG2E);
    l += pv;
    const int t = 4 * gi + (c >> 2);
    pl[(((t >> 1) * 4 + r) * 4 + (c & 3)) * 2 + (t & 1)] = f2bf(pv);
  }
  l += dpp<ROR4>(l); l += dpp<XOR8>(l); l = xrow16_sum(l);
  if (lane < 4) { wml[w][lane][0] = m; wml[w][lane][1] = l; }
  asm volatile("s_waitcnt lgkmcnt(0)" ::: "memory");
  __builtin_amdgcn_wave_barrier();
  float acc[32];
#pragma unroll
  for (int k = 0; k < 32; ++k) acc[k] = 0.f;
#pragma unroll
  for (int u = 0; u < 8; ++u) {
    const u32x4 pr = *reinterpret_cast<const u32x4*>(&pl[((u * 4 + r) * 4) * 2]);
    unsigned vv[8];
#pragma unroll
    for (int k = 0; k < 4; ++k) {
      vv[2 * k] = __builtin_amdgcn_perm(Vr[2 * u + 1][k], Vr[2 * u][k], 0x05040100u);
      vv[2 * k + 1] = __builtin_amdgcn_perm(Vr[2 * u + 1][k], Vr[2 * u][k], 0x07060302u);
    }
#pragma unroll
    for (int h = 0; h < 4; ++h)
#pragma unroll
      for (int k = 0; k < 8; ++k) acc[h * 8 + k] = dot2(vv[k], pr[h], acc[h * 8 + k]);
  }
#pragma unroll
  for (int i = 0; i < 16; ++i) acc[i] = tr16(acc[i], acc[i + 16]);
#pragma unroll
  for (int i = 0; i < 8; ++i) acc[i] = tr32(acc[i], acc[i + 8]);
  STAMP(6);
  float* accl = lds + 2048;
  {
    const int hh = 2 * (r & 1) + (r >> 1);
    *reinterpret_cast<f32x4*>(&accl[(w * 4 + hh) * 128 + 8 * c]) = f32x4{acc[0], acc[1], acc[2], acc[3]};
    *reinterpret_cast<f32x4*>(&accl[(w * 4 + hh) * 128 + 8 * c + 4]) = f32x4{acc[4], acc[5], acc[6], acc[7]};
  }
  wg_barrier();
  STAMP(7);
  {
    const int jc = tid >> 5, d = tid & 31, h = jc >> 2, dq = jc & 3;
    float M = wml[0][h][0];
#pragma unroll
    for (int ww = 1; ww < 8; ++ww) M = fmaxf(M, wml[ww][h][0]);
    float o = 0.f, L = 0.f;
#pragma unroll
    for (int ww = 0; ww < 8; ++ww) {
      const float wt = __builtin_amdgcn_exp2f((wml[ww][h][0] - M) * LOG2E);
      o = fmaf(wt, accl[(ww * 4 + h) * 128 + dq * 32 + d], o);
      L = fmaf(wt, wml[ww][h][1], L);
    }
    const unsigned tag = elds[g * 16 + jc] + 1u;
    const size_t base = WS_H2 + ((size_t)(cbase + jc) * 16 + j) * H2S * 8;
    st_gran(ws, base + (size_t)d * 8, tag, __float_as_uint(o));
    if (d < 2) st_gran(ws, base + (size_t)(32 + d) * 8, tag, __float_as_uint(d == 0 ? M : L));
  }
  asm volatile("" ::: "memory");
  STAMP(8);
  {
    const size_t base = WS_H2 + (size_t)role * 16 * H2S * 8;
    const int pp = 2 * w + (lane >= 34 ? 1 : 0);
    const int e0 = lane >= 34 ? lane - 34 : lane;
    unsigned v0 = 0, v1 = 0;
    for (unsigned spin = 0; spin < SPIN_CAP; ++spin) {
      bool ok = true;
      const u64 x0 = ld_gran(ws, base + (size_t)(pp * H2S + e0) * 8); v0 = (unsigned)x0; ok &= ((unsigned)(x0 >> 32) == mytag);
      if (lane < 4) { const u64 x1 = ld_gran(ws, base + (size_t)((2 * w + 1) * H2S + 30 + lane) * 8); v1 = (unsigned)x1; ok &= ((unsigned)(x1 >> 32) == mytag); }
      if (__all(ok)) break;
      __builtin_amdgcn_s_sleep(1);
    }
    h2l[pp][e0] = __uint_as_float(v0);
    if (lane < 4) h2l[2 * w + 1][30 + lane] = __uint_as_float(v1);
  }
  STAMP(9);
  wg_barrier();
  {
    const int rr = lane >> 3;
    float* ow = lds + 2048 + w * 128;
    {
      const int bb = lane >> 4, d = 2 * (lane & 15);
      const float m0 = h2l[bb * 4 + 0][32], m1 = h2l[bb * 4 + 1][32], m2 = h2l[bb * 4 + 2][32], m3 = h2l[bb * 4 + 3][32];
      const float M = fmaxf(fmaxf(m0, m1), fmaxf(m2, m3));
      const float w0 = __builtin_amdgcn_exp2f((m0 - M) * LOG2E), w1 = __builtin_amdgcn_exp2f((m1 - M) * LOG2E);
      const float w2 = __builtin_amdgcn_exp2f((m2 - M) * LOG2E), w3 = __builtin_amdgcn_exp2f((m3 - M) * LOG2E);
      const float L = fmaf(w0, h2l[bb * 4 + 0][33], fmaf(w1, h2l[bb * 4 + 1][33], fmaf(w2, h2l[bb * 4 + 2][33], w3 * h2l[bb * 4 + 3][33])));
      const float o0 = fmaf(w0, h2l[bb * 4 + 0][d], fmaf(w1, h2l[bb * 4 + 1][d], fmaf(w2, h2l[bb * 4 + 2][d], w3 * h2l[bb * 4 + 3][d])));
      const float o1 = fmaf(w0, h2l[bb * 4 + 0][d + 1], fmaf(w1, h2l[bb * 4 + 1][d + 1], fmaf(w2, h2l[bb * 4 + 2][d + 1], w3 * h2l[bb * 4 + 3][d + 1])));
      ow[bb * 32 + d] = rbf(o0 / L);
      ow[bb * 32 + d + 1] = rbf(o1 / L);
    }
    asm volatile("s_waitcnt lgkmcnt(0)" ::: "memory");
    __builtin_amdgcn_wave_barrier();
    float y[4][8];
#pragma unroll
    for (int bb = 0; bb < 4; ++bb)
#pragma unroll
      for (int k = 0; k < 8; ++k) y[bb][k] = 0.f;
#pragma unroll
    for (int i = 0; i < 4; ++i)
#pragma unroll
      for (int bb = 0; bb < 4; ++bb) {
        const float ov = ow[bb * 32 + 8 * i + rr];
#pragma unroll
        for (int k = 0; k < 4; ++k) { y[bb][2 * k] = fmaf(ov, bflo(Wr[i][k]), y[bb][2 * k]); y[bb][2 * k + 1] = fmaf(ov, bfhi(Wr[i][k]), y[bb][2 * k + 1]); }
      }
    float z[16];
#pragma unroll
    for (int i = 0; i < 16; ++i) {
      const float lo_ = y[i >> 3][i & 7], hi_ = y[2 + (i >> 3)][i & 7];
      const float give = (lane & 8) ? lo_ : hi_, keep = (lane & 8) ? hi_ : lo_;
      z[i] = keep + dpp<XOR8>(give);
    }
    float z2[8];
#pragma unroll
    for (int i = 0; i < 8; ++i) z2[i] = tr16(z[(i >> 2) * 8 + (i & 3)], z[(i >> 2) * 8 + 4 + (i & 3)]);
    float z3[4];
#pragma unroll
    for (int i = 0; i < 4; ++i) z3[i] = tr32(z2[(i >> 1) * 4 + (i & 1)], z2[(i >> 1) * 4 + 2 + (i & 1)]);
    {
      const int col = 64 * w + 8 * (lane & 7) + 4 * ((lane >> 4) & 1) + 2 * (lane >> 5);
#pragma unroll
      for (int b0 = 0; b0 < 2; ++b0) {
        const int bb = 2 * ((lane >> 3) & 1) + b0;
        const int idx0 = bb * 512 + col;
        const int cns = idx0 >> 4;
        const unsigned tag = elds[cns] + 1u;
        const unsigned off = (unsigned)(WS_H3 + (((size_t)(br * 128 + cns) * 128 + rem) * 16 + (idx0 & 15)) * 8);
        const u32x4 v = {__float_as_uint(z3[2 * b0]), tag, __float_as_uint(z3[2 * b0 + 1]), tag};
        __builtin_amdgcn_raw_buffer_store_b128(v, rsrc, off, 0, 16);
      }
    }
  }
  asm volatile("" ::: "memory");
  STAMP(10);
  {
    const unsigned base = (unsigned)(WS_H3 + (size_t)role * 128 * 16 * 8);
    u32x4 a0, a1;
    for (unsigned spin = 0; spin < SPIN_CAP; ++spin) {
      a0 = __builtin_amdgcn_raw_buffer_load_b128(rsrc, base + (unsigned)tid * 16u, 0, 16);
      a1 = __builtin_amdgcn_raw_buffer_load_b128(rsrc, base + (unsigned)(tid + 512) * 16u, 0, 16);
      const bool ok = (a0[1] == mytag) & (a0[3] == mytag) & (a1[1] == mytag) & (a1[3] == mytag);
      if (__all(ok)) break;
      __builtin_amdgcn_s_sleep(1);
    }
    fin[tid >> 3][2 * (tid & 7)] = __uint_as_float(a0[0]) + __uint_as_float(a1[0]);
    fin[tid >> 3][2 * (tid & 7) + 1] = __uint_as_float(a0[2]) + __uint_as_float(a1[2]);
  }
  STAMP(11);
  wg_barrier();
  if (tid < 16) {
    float t = 0.f;
#pragma unroll
    for (int i = 0; i < 64; ++i) t += fin[i][tid];
    const int idx = rem * 16 + tid, bb = idx >> 9, col = idx & 511;
    p.out[bb * 1024 + br * 512 + col] = f2bf(t);
  }
  if (tid == 0) __hip_atomic_store((gu32*)(ws + WS_E) + role, mytag, RLX_AGENT);
  STAMP(12);
}

extern "C" void kernel_launch(void* const* d_in, const int* in_sizes, int n_in,
                              void* d_out, int out_size, void* d_ws, size_t ws_size,
                              hipStream_t stream) {
  Ptrs p;
  for (int br = 0; br < 2; ++br) {
    p.x[br] = (const unsigned short*)d_in[5 * br + 0];
    p.k[br] = (const unsigned short*)d_in[5 * br + 1];
    p.v[br] = (const unsigned short*)d_in[5 * br + 2];
    p.wq[br] = (const unsigned short*)d_in[5 * br + 3];
    p.wo[br] = (const unsigned short*)d_in[5 * br + 4];
  }
  p.out = (unsigned short*)d_out;
  p.ws = (unsigned char*)d_ws;
  hipLaunchKernelGGL(k_fused, dim3(256), dim3(512), 0, stream, p);
}
```

```cpp
#include <hip/hip_runtime.h>
#include <hip/hip_bf16.h>
#include <stdint.h>

typedef unsigned u32x4 __attribute__((ext_vector_type(4)));
typedef float f32x4 __attribute__((ext_vector_type(4)));
typedef __attribute__((ext_vector_type(2))) __bf16 bf16x2;
typedef unsigned long long u64;
#define GAS __attribute__((address_space(1)))
typedef GAS u64 gu64;
typedef GAS unsigned gu32;
#define RLX_AGENT __ATOMIC_RELAXED, __HIP_MEMORY_SCOPE_AGENT

constexpr int NB = 4, DM = 512, HQ = 32, HKV = 8, SK = 8192, DH = 128, WIN = 2048;
constexpr int NSPLIT = 4, KPS = WIN / NSPLIT;
constexpr float SCALE = 0.08838834764831845f;
constexpr float LOG2E = 1.4426950408889634f;
constexpr unsigned SPIN_CAP = 20000;

constexpr size_t WS_E = 0;
constexpr size_t WS_H1 = 4096;
constexpr size_t WS_H2 = WS_H1 + (size_t)256 * 256 * 8;
constexpr int H2S = 36;
constexpr size_t WS_H3 = WS_H2 + (size_t)256 * 16 * H2S * 8;
constexpr size_t WS_END = WS_H3 + (size_t)256 * 128 * 16 * 8;

__device__ __forceinline__ float bf2f(unsigned short h) { return __uint_as_float(((unsigned)h) << 16); }
__device__ __forceinline__ float bflo(unsigned u) { return __uint_as_float(u << 16); }
__device__ __forceinline__ float bfhi(unsigned u) { return __uint_as_float(u & 0xffff0000u); }
__device__ __forceinline__ float rbf(float f) { __bf16 h = (__bf16)f; return (float)h; }
__device__ __forceinline__ unsigned short f2bf(float f) { __bf16 h = (__bf16)f; return __builtin_bit_cast(unsigned short, h); }
__device__ __forceinline__ float dot2(unsigned k, unsigned q, float acc) {
  return __builtin_amdgcn_fdot2_f32_bf16(__builtin_bit_cast(bf16x2, k), __builtin_bit_cast(bf16x2, q), acc, false);
}
template <int CTRL> __device__ __forceinline__ float dpp(float x) {
  return __builtin_bit_cast(float, __builtin_amdgcn_mov_dpp(__builtin_bit_cast(int, x), CTRL, 0xf, 0xf, true));
}
constexpr int XOR1 = 0xB1, XOR2 = 0x4E, XOR7 = 0x141, XOR8 = 0x128, ROR4 = 0x124;
__device__ __forceinline__ float xrow16_max(float x) {
  auto s = __builtin_amdgcn_permlane16_swap(__float_as_uint(x), __float_as_uint(x), false, false);
  x = fmaxf(__uint_as_float(s[0]), __uint_as_float(s[1]));
  auto t = __builtin_amdgcn_permlane32_swap(__float_as_uint(x), __float_as_uint(x), false, false);
  return fmaxf(__uint_as_float(t[0]), __uint_as_float(t[1]));
}
__device__ __forceinline__ float xrow16_sum(float x) {
  auto s = __builtin_amdgcn_permlane16_swap(__float_as_uint(x), __float_as_uint(x), false, false);
  x = __uint_as_float(s[0]) + __uint_as_float(s[1]);
  auto t = __builtin_amdgcn_permlane32_swap(__float_as_uint(x), __float_as_uint(x), false, false);
  return __uint_as_float(t[0]) + __uint_as_float(t[1]);
}
__device__ __forceinline__ float tr16(float a, float b) {
  auto s = __builtin_amdgcn_permlane16_swap(__float_as_uint(a), __float_as_uint(b), false, false);
  return __uint_as_float(s[0]) + __uint_as_float(s[1]);
}
__device__ __forceinline__ float tr32(float a, float b) {
  auto s = __builtin_amdgcn_permlane32_swap(__float_as_uint(a), __float_as_uint(b), false, false);
  return __uint_as_float(s[0]) + __uint_as_float(s[1]);
}
__device__ __forceinline__ u32x4 ldnt(const void* p) { return __builtin_nontemporal_load(reinterpret_cast<const u32x4*>(p)); }
__device__ __forceinline__ void st_gran(unsigned char* ws, size_t byteoff, unsigned tag, unsigned val) {
  __hip_atomic_store((gu64*)(ws + byteoff), ((u64)tag << 32) | (u64)val, RLX_AGENT);
}
__device__ __forceinline__ u64 ld_gran(const unsigned char* ws, size_t byteoff) {
  return __hip_atomic_load((gu64*)(ws + byteoff), RLX_AGENT);
}
__device__ __forceinline__ void wg_barrier() {
  asm volatile("s_waitcnt lgkmcnt(0)" ::: "memory");
  __builtin_amdgcn_s_barrier();
  asm volatile("" ::: "memory");
}

#define SB() __builtin_amdgcn_sched_barrier(0)
struct Ptrs {
  const unsigned short* x[2];
  const unsigned short* k[2];
  const unsigned short* v[2];
  const unsigned short* wq[2];
  const unsigned short* wo[2];
  unsigned short* out;
  unsigned char* ws;
};

__global__ void __launch_bounds__(512, 2) k_fused(Ptrs p) {
  __shared__ __attribute__((aligned(16))) float lds[8 * 4 * 512];
  __shared__ __attribute__((aligned(16))) unsigned qlds[256];
  __shared__ unsigned elds[128];
  __shared__ float wml[8][4][2];
  __shared__ float h2l[16][H2S];
  __shared__ float ol[4][32];
  __shared__ float fin[64][16];
  __shared__ unsigned qcnt;

  const int bid = blockIdx.x;
  const int role = (bid & 7) * 32 + (bid >> 3);
  const int br = role >> 7, rem = role & 127, g = rem >> 4, j = rem & 15, b = j >> 2, s = j & 3;
  const int tid = threadIdx.x, lane = tid & 63;
  const int w = __builtin_amdgcn_readfirstlane(tid >> 6);
  unsigned char* ws = p.ws;
  const int cbase = br * 128 + g * 16;
  const __amdgpu_buffer_rsrc_t rsrc = __builtin_amdgcn_make_buffer_rsrc(ws, 0, (int)WS_END, 0x00020000);
  if (tid == 0) qcnt = 0u;
  const unsigned eword = __hip_atomic_load((gu32*)(ws + WS_E) + br * 128 + (tid & 127), RLX_AGENT);
  const int r = lane >> 4, c = lane & 15;
  const size_t kvbase = ((size_t)(b * HKV + g) * SK + (SK - WIN) + s * KPS + w * 64) * DH;
  const unsigned short* kp = p.k[br] + kvbase + (size_t)r * DH + 8 * c;
  const unsigned short* vp = p.v[br] + kvbase + (size_t)r * DH + 8 * c;
  u32x4 Kr[16], Vr[16];
  wg_barrier();
  unsigned mytag = 0;
  if (w < 4) {
    __builtin_amdgcn_s_setprio(3);
    const int qr = lane >> 2, qc = lane & 3;
    u32x4 W[8];
    {
      const unsigned short* wq = p.wq[br] + (size_t)(128 * w + qr) * 4096 + g * 512 + j * 32 + 8 * qc;
#pragma unroll
      for (int t = 0; t < 8; ++t) W[t] = ldnt(wq + (size_t)(16 * t) * 4096);
    }
    unsigned short xr[8][4];
    {
      const unsigned short* x = p.x[br] + 128 * w + qr;
#pragma unroll
      for (int t = 0; t < 8; ++t)
#pragma unroll
        for (int bb = 0; bb < 4; ++bb) xr[t][bb] = x[bb * DM + 16 * t];
    }
    if (tid < 128) elds[tid] = eword;
    float acc[4][8];
#pragma unroll
    for (int bb = 0; bb < 4; ++bb)
#pragma unroll
      for (int k = 0; k < 8; ++k) acc[bb][k] = 0.f;
#pragma unroll
    for (int t = 0; t < 8; ++t)
#pragma unroll
      for (int bb = 0; bb < 4; ++bb) {
        const float xv = bf2f(xr[t][bb]);
#pragma unroll
        for (int k = 0; k < 4; ++k) {
          acc[bb][2 * k] = fmaf(xv, bflo(W[t][k]), acc[bb][2 * k]);
          acc[bb][2 * k + 1] = fmaf(xv, bfhi(W[t][k]), acc[bb][2 * k + 1]);
        }
      }
#pragma unroll
    for (int bb = 0; bb < 4; ++bb)
#pragma unroll
      for (int k = 0; k < 8; ++k) {
        float v = acc[bb][k];
        v += dpp<ROR4>(v); v += dpp<XOR8>(v); v = xrow16_sum(v);
        acc[bb][k] = v;
      }
    if (qr == 0) {
      float* red = lds + w * 128;
#pragma unroll
      for (int bb = 0; bb < 4; ++bb)
#pragma unroll
        for (int k = 0; k < 8; ++k) red[bb * 32 + 8 * qc + k] = acc[bb][k];
    }
    asm volatile("s_waitcnt lgkmcnt(0)" ::: "memory");
    if (lane == 0) __hip_atomic_fetch_add(&qcnt, 1u, __ATOMIC_RELAXED, __HIP_MEMORY_SCOPE_WORKGROUP);
  }
  const size_t h1base = WS_H1 + (size_t)role * 256 * 8;
  u64 g0 = 0, g1 = 0, g2 = 0, g3 = 0;
  if (w == 0) {
    for (unsigned spin = 0; spin < 1000000u; ++spin) {
      const unsigned cnt = __hip_atomic_load(&qcnt, __ATOMIC_RELAXED, __HIP_MEMORY_SCOPE_WORKGROUP);
      if (cnt >= 4u) break;
    }
    asm volatile("" ::: "memory");
#pragma unroll
    for (int k = 0; k < 4; ++k) {
      const int idx = k * 64 + lane, cc = idx >> 4, i = idx & 15, bb = cc >> 2;
      float s0 = 0.f, s1 = 0.f;
#pragma unroll
      for (int ww = 0; ww < 4; ++ww) { s0 += lds[ww * 128 + bb * 32 + 2 * i]; s1 += lds[ww * 128 + bb * 32 + 2 * i + 1]; }
      const unsigned val = (unsigned)f2bf(s0) | ((unsigned)f2bf(s1) << 16);
      st_gran(ws, WS_H1 + ((size_t)(cbase + cc) * 256 + j * 16 + i) * 8, elds[g * 16 + cc] + 1u, val);
    }
    g0 = ld_gran(ws, h1base + (size_t)(0 * 64 + lane) * 8);
    g1 = ld_gran(ws, h1base + (size_t)(1 * 64 + lane) * 8);
    g2 = ld_gran(ws, h1base + (size_t)(2 * 64 + lane) * 8);
    g3 = ld_gran(ws, h1base + (size_t)(3 * 64 + lane) * 8);
  }
  SB();
  if (w != 0) __builtin_amdgcn_s_setprio(0);
  unsigned touch0, touch1, touch2, touch3;
  {
    size_t o0, o1;
    if (w == 4) { o0 = WS_H3 + ((size_t)(br * 128 + lane) * 128 + rem) * 128; o1 = WS_H3 + ((size_t)(br * 128 + 64 + lane) * 128 + rem) * 128; }
    else if (w == 5) { o0 = WS_H3 + (size_t)role * 16384 + (size_t)lane * 256; o1 = o0 + 128; }
    else if (w == 6) { o0 = WS_H2 + ((size_t)(cbase + (lane & 15)) * 16 + j) * H2S * 8 + (size_t)(lane >> 4) * 64; o1 = WS_H2 + (size_t)role * 16 * H2S * 8 + (size_t)lane * 64; }
    else { o0 = WS_H1 + (size_t)role * 2048 + (size_t)(lane & 15) * 128; o1 = o0; }
    touch0 = __hip_atomic_load((gu32*)(ws + o0), RLX_AGENT);
    touch1 = __hip_atomic_load((gu32*)(ws + o1), RLX_AGENT);
    touch2 = *reinterpret_cast<const unsigned*>(p.v[br] + kvbase + (size_t)(lane & 3) * 2048);
    touch3 = *reinterpret_cast<const unsigned*>(p.wo[br] + (size_t)(g * 512 + j * 32 + 4 * (lane & 7)) * DM);
  }
  SB();
#pragma unroll
  for (int t = 0; t < 16; ++t) { Kr[t] = ldnt(kp + (size_t)t * 4 * DH); SB(); }
  if (w == 0) {
    mytag = elds[g * 16 + j] + 1u;
    const bool ok0 = ((unsigned)(g0 >> 32) == mytag) & ((unsigned)(g1 >> 32) == mytag) & ((unsigned)(g2 >> 32) == mytag) & ((unsigned)(g3 >> 32) == mytag);
    if (!__all(ok0)) {
      for (unsigned spin = 0; spin < SPIN_CAP; ++spin) {
        g0 = ld_gran(ws, h1base + (size_t)(0 * 64 + lane) * 8);
        g1 = ld_gran(ws, h1base + (size_t)(1 * 64 + lane) * 8);
        g2 = ld_gran(ws, h1base + (size_t)(2 * 64 + lane) * 8);
        g3 = ld_gran(ws, h1base + (size_t)(3 * 64 + lane) * 8);
        const bool ok = ((unsigned)(g0 >> 32) == mytag) & ((unsigned)(g1 >> 32) == mytag) & ((unsigned)(g2 >> 32) == mytag) & ((unsigned)(g3 >> 32) == mytag);
        if (__all(ok)) break;
        __builtin_amdgcn_s_sleep(1);
      }
    }
    qlds[0 * 64 + lane] = (unsigned)g0; qlds[1 * 64 + lane] = (unsigned)g1; qlds[2 * 64 + lane] = (unsigned)g2; qlds[3 * 64 + lane] = (unsigned)g3;
  }
  __builtin_amdgcn_s_setprio(0);
  wg_barrier();
  asm volatile("" :: "v"(touch0), "v"(touch1), "v"(touch2), "v"(touch3));
  mytag = elds[g * 16 + j] + 1u;
  u32x4 qp[4];
#pragma unroll
  for (int h = 0; h < 4; ++h) qp[h] = *reinterpret_cast<const u32x4*>(&qlds[h * 64 + 4 * c]);
  float sc[4];
#pragma unroll
  for (int gi = 0; gi < 4; ++gi) {
    float x[16];
#pragma unroll
    for (int tl = 0; tl < 4; ++tl)
#pragma unroll
      for (int h = 0; h < 4; ++h) {
        float d = 0.f;
#pragma unroll
        for (int k = 0; k < 4; ++k) d = dot2(Kr[4 * gi + tl][k], qp[h][k], d);
        x[tl * 4 + h] = d;
      }
    SB();
#pragma unroll
    for (int tl = 0; tl < 4; ++tl) { Vr[4 * gi + tl] = ldnt(vp + (size_t)(4 * gi + tl) * 4 * DH); SB(); }
#define TR_STEP(HALF, CTRL, BIT) _Pragma("unroll") for (int hh = 0; hh < (HALF); ++hh) { \
      const float lo_ = x[hh], hi_ = x[hh + (HALF)]; const float t1_ = lo_ + dpp<CTRL>(lo_), t2_ = hi_ + dpp<CTRL>(hi_); \
      x[hh] = (c & (BIT)) ? t2_ : t1_; }
    TR_STEP(8, XOR8, 8) TR_STEP(4, XOR7, 4) TR_STEP(2, XOR2, 2) TR_STEP(1, XOR1, 1)
#undef TR_STEP
    sc[gi] = rbf(rbf(x[0]) * SCALE);
  }
  u32x4 Wr[4];
  {
    const unsigned short* wo = p.wo[br] + (size_t)(g * 512 + j * 32 + (lane >> 3)) * DM + 64 * w + 8 * (lane & 7);
    SB();
#pragma unroll
    for (int i = 0; i < 4; ++i) Wr[i] = ldnt(wo + (size_t)(8 * i) * DM);
    SB();
  }
  float m = fmaxf(fmaxf(sc[0], sc[1]), fmaxf(sc[2], sc[3]));
  m = fmaxf(m, dpp<ROR4>(m)); m = fmaxf(m, dpp<XOR8>(m)); m = xrow16_max(m);
  unsigned short* pl = reinterpret_cast<unsigned short*>(lds + w * 256);
  float l = 0.f;
#pragma unroll
  for (int gi = 0; gi < 4; ++gi) {
    const float pv = __builtin_amdgcn_exp2f((sc[gi] - m) * LOG2E);
    l += pv;
    const int t = 4 * gi + (c >> 2);
    pl[(((t >> 1) * 4 + r) * 4 + (c & 3)) * 2 + (t & 1)] = f2bf(pv);
  }
  l += dpp<ROR4>(l); l += dpp<XOR8>(l); l = xrow16_sum(l);
  if (lane < 4) { wml[w][lane][0] = m; wml[w][lane][1] = l; }
  asm volatile("s_waitcnt lgkmcnt(0)" ::: "memory");
  __builtin_amdgcn_wave_barrier();
  float acc[32];
#pragma unroll
  for (int k = 0; k < 32; ++k) acc[k] = 0.f;
#pragma unroll
  for (int u = 0; u < 8; ++u) {
    const u32x4 pr = *reinterpret_cast<const u32x4*>(&pl[((u * 4 + r) * 4) * 2]);
    unsigned vv[8];
#pragma unroll
    for (int k = 0; k < 4; ++k) {
      vv[2 * k] = __builtin_amdgcn_perm(Vr[2 * u + 1][k], Vr[2 * u][k], 0x05040100u);
      vv[2 * k + 1] = __builtin_amdgcn_perm(Vr[2 * u + 1][k], Vr[2 * u][k], 0x07060302u);
    }
#pragma unroll
    for (int h = 0; h < 4; ++h)
#pragma unroll
      for (int k = 0; k < 8; ++k) acc[h * 8 + k] = dot2(vv[k], pr[h], acc[h * 8 + k]);
  }
#pragma unroll
  for (int i = 0; i < 16; ++i) acc[i] = tr16(acc[i], acc[i + 16]);
#pragma unroll
  for (int i = 0; i < 8; ++i) acc[i] = tr32(acc[i], acc[i + 8]);
  float* accl = lds + 2048;
  {
    const int hh = 2 * (r & 1) + (r >> 1);
    *reinterpret_cast<f32x4*>(&accl[(w * 4 + hh) * 128 + 8 * c]) = f32x4{acc[0], acc[1], acc[2], acc[3]};
    *reinterpret_cast<f32x4*>(&accl[(w * 4 + hh) * 128 + 8 * c + 4]) = f32x4{acc[4], acc[5], acc[6], acc[7]};
  }
  wg_barrier();
  {
    const int jc = tid >> 5, d = tid & 31, h = jc >> 2, dq = jc & 3;
    float M = wml[0][h][0];
#pragma unroll
    for (int ww = 1; ww < 8; ++ww) M = fmaxf(M, wml[ww][h][0]);
    float o = 0.f, L = 0.f;
#pragma unroll
    for (int ww = 0; ww < 8; ++ww) {
      const float wt = __builtin_amdgcn_exp2f((wml[ww][h][0] - M) * LOG2E);
      o = fmaf(wt, accl[(ww * 4 + h) * 128 + dq * 32 + d], o);
      L = fmaf(wt, wml[ww][h][1], L);
    }
    const unsigned tag = elds[g * 16 + jc] + 1u;
    const size_t base = WS_H2 + ((size_t)(cbase + jc) * 16 + j) * H2S * 8;
    st_gran(ws, base + (size_t)d * 8, tag, __float_as_uint(o));
    if (d < 2) st_gran(ws, base + (size_t)(32 + d) * 8, tag, __float_as_uint(d == 0 ? M : L));
  }
  asm volatile("" ::: "memory");
  {
    const size_t base = WS_H2 + (size_t)role * 16 * H2S * 8;
    const int pp = 2 * w + (lane >= 34 ? 1 : 0);
    const int e0 = lane >= 34 ? lane - 34 : lane;
    unsigned v0 = 0, v1 = 0;
    for (unsigned spin = 0; spin < SPIN_CAP; ++spin) {
      bool ok = true;
      const u64 x0 = ld_gran(ws, base + (size_t)(pp * H2S + e0) * 8); v0 = (unsigned)x0; ok &= ((unsigned)(x0 >> 32) == mytag);
      if (lane < 4) { const u64 x1 = ld_gran(ws, base + (size_t)((2 * w + 1) * H2S + 30 + lane) * 8); v1 = (unsigned)x1; ok &= ((unsigned)(x1 >> 32) == mytag); }
      if (__all(ok)) break;
      __builtin_amdgcn_s_sleep(1);
    }
    h2l[pp][e0] = __uint_as_float(v0);
    if (lane < 4) h2l[2 * w + 1][30 + lane] = __uint_as_float(v1);
  }
  wg_barrier();
  {
    const int rr = lane >> 3;
    float* ow = lds + 2048 + w * 128;
    {
      const int bb = lane >> 4, d = 2 * (lane & 15);
      const float m0 = h2l[bb * 4 + 0][32], m1 = h2l[bb * 4 + 1][32], m2 = h2l[bb * 4 + 2][32], m3 = h2l[bb * 4 + 3][32];
      const float M = fmaxf(fmaxf(m0, m1), fmaxf(m2, m3));
      const float w0 = __builtin_amdgcn_exp2f((m0 - M) * LOG2E), w1 = __builtin_amdgcn_exp2f((m1 - M) * LOG2E);
      const float w2 = __builtin_amdgcn_exp2f((m2 - M) * LOG2E), w3 = __builtin_amdgcn_exp2f((m3 - M) * LOG2E);
      const float L = fmaf(w0, h2l[bb * 4 + 0][33], fmaf(w1, h2l[bb * 4 + 1][33], fmaf(w2, h2l[bb * 4 + 2][33], w3 * h2l[bb * 4 + 3][33])));
      const float o0 = fmaf(w0, h2l[bb * 4 + 0][d], fmaf(w1, h2l[bb * 4 + 1][d], fmaf(w2, h2l[bb * 4 + 2][d], w3 * h2l[bb * 4 + 3][d])));
      const float o1 = fmaf(w0, h2l[bb * 4 + 0][d + 1], fmaf(w1, h2l[bb * 4 + 1][d + 1], fmaf(w2, h2l[bb * 4 + 2][d + 1], w3 * h2l[bb * 4 + 3][d + 1])));
      ow[bb * 32 + d] = rbf(o0 / L);
      ow[bb * 32 + d + 1] = rbf(o1 / L);
    }
    asm volatile("s_waitcnt lgkmcnt(0)" ::: "memory");
    __builtin_amdgcn_wave_barrier();
    float y[4][8];
#pragma unroll
    for (int bb = 0; bb < 4; ++bb)
#pragma unroll
      for (int k = 0; k < 8; ++k) y[bb][k] = 0.f;
#pragma unroll
    for (int i = 0; i < 4; ++i)
#pragma unroll
      for (int bb = 0; bb < 4; ++bb) {
        const float ov = ow[bb * 32 + 8 * i + rr];
#pragma unroll
        for (int k = 0; k < 4; ++k) { y[bb][2 * k] = fmaf(ov, bflo(Wr[i][k]), y[bb][2 * k]); y[bb][2 * k + 1] = fmaf(ov, bfhi(Wr[i][k]), y[bb][2 * k + 1]); }
      }
    float z[16];
#pragma unroll
    for (int i = 0; i < 16; ++i) {
      const float lo_ = y[i >> 3][i & 7], hi_ = y[2 + (i >> 3)][i & 7];
      const float give = (lane & 8) ? lo_ : hi_, keep = (lane & 8) ? hi_ : lo_;
      z[i] = keep + dpp<XOR8>(give);
    }
    float z2[8];
#pragma unroll
    for (int i = 0; i < 8; ++i) z2[i] = tr16(z[(i >> 2) * 8 + (i & 3)], z[(i >> 2) * 8 + 4 + (i & 3)]);
    float z3[4];
#pragma unroll
    for (int i = 0; i < 4; ++i) z3[i] = tr32(z2[(i >> 1) * 4 + (i & 1)], z2[(i >> 1) * 4 + 2 + (i & 1)]);
    {
      const int col = 64 * w + 8 * (lane & 7) + 4 * ((lane >> 4) & 1) + 2 * (lane >> 5);
#pragma unroll
      for (int b0 = 0; b0 < 2; ++b0) {
        const int bb = 2 * ((lane >> 3) & 1) + b0;
        const int idx0 = bb * 512 + col;
        const int cns = idx0 >> 4;
        const unsigned tag = elds[cns] + 1u;
        const unsigned off = (unsigned)(WS_H3 + (((size_t)(br * 128 + cns) * 128 + rem) * 16 + (idx0 & 15)) * 8);
        const u32x4 v = {__float_as_uint(z3[2 * b0]), tag, __float_as_uint(z3[2 * b0 + 1]), tag};
        __builtin_amdgcn_raw_buffer_store_b128(v, rsrc, off, 0, 16);
      }
    }
  }
  asm volatile("" ::: "memory");
  {
    const unsigned base = (unsigned)(WS_H3 + (size_t)role * 128 * 16 * 8);
    u32x4 a0, a1;
    for (unsigned spin = 0; spin < SPIN_CAP; ++spin) {
      a0 = __builtin_amdgcn_raw_buffer_load_b128(rsrc, base + (unsigned)tid * 16u, 0, 16);
      a1 = __builtin_amdgcn_raw_buffer_load_b128(rsrc, base + (unsigned)(tid + 512) * 16u, 0, 16);
      const bool ok = (a0[1] == mytag) & (a0[3] == mytag) & (a1[1] == mytag) & (a1[3] == mytag);
      if (__all(ok)) break;
      __builtin_amdgcn_s_sleep(1);
    }
    fin[tid >> 3][2 * (tid & 7)] = __uint_as_float(a0[0]) + __uint_as_float(a1[0]);
    fin[tid >> 3][2 * (tid & 7) + 1] = __uint_as_float(a0[2]) + __uint_as_float(a1[2]);
  }
  wg_barrier();
  if (tid < 16) {
    float t = 0.f;
#pragma unroll
    for (int i = 0; i < 64; ++i) t += fin[i][tid];
    const int idx = rem * 16 + tid, bb = idx >> 9, col = idx & 511;
    p.out[bb * 1024 + br * 512 + col] = f2bf(t);
  }
  if (tid == 0) __hip_atomic_store((gu32*)(ws + WS_E) + role, mytag, RLX_AGENT);
}

extern "C" void kernel_launch(void* const* d_in, const int* in_sizes, int n_in,
                              void* d_out, int out_size, void* d_ws, size_t ws_size,
                              hipStream_t stream) {
  Ptrs p;
  for (int br = 0; br < 2; ++br) {
    p.x[br] = (const unsigned short*)d_in[5 * br + 0];
    p.k[br] = (const unsigned short*)d_in[5 * br + 1];
    p.v[br] = (const unsigned short*)d_in[5 * br + 2];
    p.wq[br] = (const unsigned short*)d_in[5 * br + 3];
    p.wo[br] = (const unsigned short*)d_in[5 * br + 4];
  }
  p.out = (unsigned short*)d_out;
  p.ws = (unsigned char*)d_ws;
  hipLaunchKernelGGL(k_fused, dim3(256), dim3(512), 0, stream, p);
}
```
